```python
import torch

def _softmax(x, axis=-1):
    return torch.softmax(x, dim=axis)
def _gelu(x):
    return torch.nn.functional.gelu(x, approximate="tanh")
def _silu(x):
    return torch.nn.functional.silu(x)
def _sigmoid(x):
    return torch.sigmoid(x)
def _layernorm(x, axis=-1, eps=1e-5):
    m = x.mean(dim=axis, keepdim=True)
    v = x.var(dim=axis, keepdim=True, unbiased=False)
    return (x - m) / torch.sqrt(v + eps)
def _scatter_add(tgt, idx, src, axis=0):
    return tgt.clone().index_add_(axis, idx, src)
def _segment_sum(src, seg, n):
    out = torch.zeros((n,) + src.shape[1:], dtype=src.dtype)
    return out.index_add_(0, seg, src)
def _pad(x, pw):
    flat = tuple(v for pair in reversed(pw) for v in pair)
    return torch.nn.functional.pad(x, flat)

kernel_name = 'interleave_layers__sliding_local_dilated__sliding_local_dilated__prompt'

def setup_inputs():

    _w = (lambda a: (lambda t: t.to(torch.bfloat16) if t.is_floating_point() else t)(torch.as_tensor(a)))
    def _A_setup():
        B, S, D = 1, 2048, 512
        return {
            "x": _w((lambda *s: torch.randn(*s, dtype=torch.bfloat16) * 0.05)(B, S, D)),
            "wqkv": _w((lambda *s: torch.randn(*s, dtype=torch.bfloat16) * 0.05)(D, 3 * D)),
            "wo": _w((lambda *s: torch.randn(*s, dtype=torch.bfloat16) * 0.05)(D, D)),
        }
    _A_in = _A_setup()

    def _B_setup():
        B, S, D = 1, 2048, 512
        return {
            "x": _w((lambda *s: torch.randn(*s, dtype=torch.bfloat16) * 0.05)(B, S, D)),
            "wqkv": _w((lambda *s: torch.randn(*s, dtype=torch.bfloat16) * 0.05)(D, 3 * D)),
            "wo": _w((lambda *s: torch.randn(*s, dtype=torch.bfloat16) * 0.05)(D, D)),
        }
    _B_in = _B_setup()

    return {
        "A_x": _A_in["x"],
        "A_wqkv": _A_in["wqkv"],
        "A_wo": _A_in["wo"],
        "B_x": _B_in["x"],
        "B_wqkv": _B_in["wqkv"],
        "B_wo": _B_in["wo"],
    }

def reference(A_x, A_wqkv, A_wo, B_x, B_wqkv, B_wo):
    def _A_ref(x, wqkv, wo):
        B, S, D = x.shape
        H = 32; Dh = D // H
        W, dil = 8, 2
        qkv = (lambda x, *d: x.permute(*d))((x @ wqkv).reshape(B, S, 3, H, Dh), 2, 0, 3, 1, 4)
        q, k, v = qkv[0], qkv[1], qkv[2]
        scale = Dh ** -0.5
        pos = torch.arange(S)
        off = torch.arange(W) * dil
        idx = pos[:, None] - off[None, :]
        mask = idx >= 0
        idx_c = torch.where(mask, idx, 0).reshape(-1)
        kg = k[:, :, idx_c, :].reshape(B, H, S, W, Dh)
        vg = v[:, :, idx_c, :].reshape(B, H, S, W, Dh)
        logits = torch.einsum("bhsd,bhswd->bhsw", q, kg) * scale
        logits = torch.where(mask[None, None, :, :], logits, -1e9)
        attn = _softmax(logits, axis=-1)
        o = torch.einsum("bhsw,bhswd->bhsd", attn, vg)
        return (lambda x, *d: x.permute(*d))(o, 0, 2, 1, 3).reshape(B, S, D) @ wo

    def _B_ref(x, wqkv, wo):
        B, S, D = x.shape
        H = 32; Dh = D // H
        W, dil = 8, 2
        qkv = (lambda x, *d: x.permute(*d))((x @ wqkv).reshape(B, S, 3, H, Dh), 2, 0, 3, 1, 4)
        q, k, v = qkv[0], qkv[1], qkv[2]
        scale = Dh ** -0.5
        pos = torch.arange(S)
        off = torch.arange(W) * dil
        idx = pos[:, None] - off[None, :]
        mask = idx >= 0
        idx_c = torch.where(mask, idx, 0).reshape(-1)
        kg = k[:, :, idx_c, :].reshape(B, H, S, W, Dh)
        vg = v[:, :, idx_c, :].reshape(B, H, S, W, Dh)
        logits = torch.einsum("bhsd,bhswd->bhsw", q, kg) * scale
        logits = torch.where(mask[None, None, :, :], logits, -1e9)
        attn = _softmax(logits, axis=-1)
        o = torch.einsum("bhsw,bhswd->bhsd", attn, vg)
        return (lambda x, *d: x.permute(*d))(o, 0, 2, 1, 3).reshape(B, S, D) @ wo

    x = A_x
    x = x + _A_ref(_layernorm(x, axis=-1), A_wqkv, A_wo)
    x = x + _B_ref(_layernorm(x, axis=-1), B_wqkv, B_wo)
    x = x + _A_ref(_layernorm(x, axis=-1), A_wqkv, A_wo)
    x = x + _B_ref(_layernorm(x, axis=-1), B_wqkv, B_wo)
    return x
```

```cpp
#include <hip/hip_runtime.h>
#include <hip/hip_bf16.h>
typedef unsigned short u16;
__device__ __forceinline__ float bf2f(u16 b){ return __uint_as_float(((unsigned)b)<<16); }
__device__ __forceinline__ u16 f2bf(float f){ unsigned u=__float_as_uint(f); u += 0x7FFFu + ((u>>16)&1u); return (u16)(u>>16); }
__device__ __forceinline__ float rbf(float f){ return bf2f(f2bf(f)); }
constexpr int S=2048, D=512;
__global__ void k_ln(const u16* x, u16* y){
  int row=blockIdx.x; int t=threadIdx.x;
  float v[8]; float s=0;
  for(int i=0;i<8;i++){ v[i]=bf2f(x[row*D+t*8+i]); s+=v[i]; }
  for(int m=32;m>=1;m>>=1) s+=__shfl_xor(s,m);
  float mean=s/D; float q=0;
  for(int i=0;i<8;i++){ float d=v[i]-mean; q+=d*d; }
  for(int m=32;m>=1;m>>=1) q+=__shfl_xor(q,m);
  float var=rbf(q/D); float mb=rbf(mean);
  float sd=rbf(sqrtf(rbf(var+1e-5f)));
  for(int i=0;i<8;i++) y[row*D+t*8+i]=f2bf(rbf(v[i]-mb)/sd);
}
__global__ void k_gemm(const u16* A,const u16* B,u16* C,const u16* R,int N,int K){
  int n=blockIdx.x*64+threadIdx.x; int m0=blockIdx.y*8;
  float acc[8]={0,0,0,0,0,0,0,0};
  for(int k=0;k<K;k++){ float b=bf2f(B[k*N+n]);
    for(int i=0;i<8;i++) acc[i]+=bf2f(A[(m0+i)*K+k])*b; }
  for(int i=0;i<8;i++){ float r=rbf(acc[i]); if(R) r=bf2f(R[(m0+i)*N+n])+r; C[(m0+i)*N+n]=f2bf(r);}
}
__global__ void k_attn(const u16* qkv,u16* o){
  int s=blockIdx.x; int h=threadIdx.x;
  float lg[8]; float mx=-1e30f;
  for(int w=0;w<8;w++){ int j=s-2*w; float a=0;
    if(j>=0){ for(int d=0;d<16;d++) a+=bf2f(qkv[s*1536+h*16+d])*bf2f(qkv[j*1536+512+h*16+d]); a=rbf(rbf(a)*0.25f);} else a=-1e9f;
    lg[w]=a; mx=fmaxf(mx,a);}
  float sum=0; for(int w=0;w<8;w++){ lg[w]=__expf(lg[w]-mx); sum+=lg[w]; }
  for(int d=0;d<16;d++){ float a=0; for(int w=0;w<8;w++){ int j=s-2*w; if(j<0) j=0; a+=rbf(lg[w]/sum)*bf2f(qkv[j*1536+1024+h*16+d]); } o[s*D+h*16+d]=f2bf(a);}
}
extern "C" void kernel_launch(void* const* d_in, const int* in_sizes, int n_in, void* d_out, int out_size, void* d_ws, size_t ws_size, hipStream_t stream){
  const u16* x0=(const u16*)d_in[0]; const u16* wq[2]={(const u16*)d_in[1],(const u16*)d_in[4]}; const u16* wo[2]={(const u16*)d_in[2],(const u16*)d_in[5]};
  u16* ws=(u16*)d_ws; u16* ln=ws; u16* qkv=ln+S*D; u16* o=qkv+S*1536; u16* xa=o+S*D; u16* xb=xa+S*D;
  const u16* cur=x0;
  for(int l=0;l<4;l++){
    k_ln<<<S,64,0,stream>>>(cur,ln);
    k_gemm<<<dim3(1536/64,S/8),64,0,stream>>>(ln,wq[l&1],qkv,nullptr,1536,512);
    k_attn<<<S,32,0,stream>>>(qkv,o);
    u16* nx = l==3 ? (u16*)d_out : (l&1? xb: xa);
    k_gemm<<<dim3(512/64,S/8),64,0,stream>>>(o,wo[l&1],nx,cur,512,512);
    cur=nx;
  }
}
```

```cpp
#include <hip/hip_runtime.h>

struct Params {
  const void* x0; const void* wq0; const void* wo0; const void* wq1; const void* wo1;
  void* out; void* ws;
};

__global__ void __launch_bounds__(256, 1) fused_kernel(Params p) {
  __shared__ __attribute__((aligned(16))) char smem[163840];
  asm volatile(
"s_mov_b32 s38, %1\n"
"s_load_dwordx8 s[80:87], %0, 0\n"
"s_load_dwordx4 s[88:91], %0, 32\n"
"s_load_dwordx2 s[92:93], %0, 48\n"
"s_getpc_b64 s[36:37]\n"
"L_pc:\n"
"v_mov_b32 v0, %2\n"
"s_bfe_u32 s34, s38, 0x10003\n"
"s_lshl_b32 s34, s34, 15\n"
"v_lshl_add_u32 v1, v0, 7, s34\n"
"v_min_u32 v1, L_end-L_pc-4, v1\n"
"global_load_dword v120, v1, s[36:37]\n"
"v_lshrrev_b32 v3, 6, v0\n"
"v_and_b32 v4, 63, v0\n"
"v_readfirstlane_b32 s32, v3\n"
"v_and_b32 v7, 15, v4\n"
"v_lshrrev_b32 v8, 4, v4\n"
"s_bfe_u32 s39, s38, 0x20001\n"
"s_lshl_b32 s39, s39, 2\n"
"s_lshr_b32 s34, s38, 6\n"
"s_add_u32 s39, s39, s34\n"
"s_bfe_u32 s58, s38, 0x30003\n"
"s_and_b32 s35, s38, 1\n"
"s_lshl_b32 s35, s35, 3\n"
"s_add_u32 s58, s58, s35\n"
"s_lshl_b32 s34, s34, 1\n"
"s_add_u32 s57, s58, s34\n"
"s_and_b32 s57, s57, 3\n"
"s_add_u32 s60, s57, 1\n"
"s_and_b32 s60, s60, 3\n"
"s_and_b32 s59, s39, 7\n"
"s_lshr_b32 s35, s39, 3\n"
"s_lshl_b32 s55, s59, 8\n"
"s_add_u32 s55, s55, s35\n"
"s_lshl_b32 s55, s55, 10\n"
"v_xor_b32 v12, v7, v8\n"
"v_lshlrev_b32 v12, 4, v12\n"
"v_lshl_add_u32 v12, v8, 11, v12\n"
"s_lshl_b32 s34, s32, 16\n"
"v_add_u32 v32, s34, v12\n"
"v_xor_b32 v33, 64, v32\n"
"v_add_u32 v33, 7168, v33\n"
"v_xor_b32 v34, 128, v32\n"
"v_add_u32 v34, 14336, v34\n"
"v_xor_b32 v35, 192, v32\n"
"v_add_u32 v35, 21504, v35\n"
"s_lshl_b32 s48, s32, 13\n"
"s_add_u32 s56, s48, 65536\n"
"s_lshl_b32 s50, s32, 11\n"
"s_add_u32 s50, s50, 98304\n"
"s_lshl_b32 s49, s60, 8\n"
"s_add_u32 s49, s49, s55\n"
"s_mul_i32 s79, s60, 393216\n"
"s_mul_i32 s78, s32, 98304\n"
"s_add_u32 s79, s79, s78\n"
"v_lshlrev_b32 v115, 4, v4\n"
"v_accvgpr_write_b32 a0, -1\n"
"v_accvgpr_write_b32 a1, -1\n"
"v_accvgpr_write_b32 a2, -1\n"
"v_accvgpr_write_b32 a3, -1\n"
"s_lshr_b32 s61, s58, 2\n"
"s_lshl_b32 s63, s39, 17\n"
"s_add_u32 s63, s63, 0\n"
"s_lshl_b32 s34, s32, 15\n"
"s_add_u32 s40, s63, s34\n"
"s_lshl_b32 s35, s58, 11\n"
"s_add_u32 s44, s40, s35\n"
"s_add_u32 s40, s40, 8388608\n"
"s_lshl_b32 s35, s61, 15\n"
"s_add_u32 s45, s63, s35\n"
"s_lshl_b32 s34, s57, 2\n"
"s_add_u32 s34, s34, s32\n"
"s_lshl_b32 s34, s34, 11\n"
"s_add_u32 s46, s45, s34\n"
"s_add_u32 s46, s46, 8388608\n"
"s_add_u32 s41, s63, s48\n"
"s_add_u32 s41, s41, 8355840\n"
"s_add_u32 s45, s45, s48\n"
"s_add_u32 s51, s48, 98304\n"
"s_lshl_b32 s34, s32, 12\n"
"s_sub_u32 s45, s45, s34\n"
"s_sub_u32 s51, s51, s34\n"
"s_lshl_b32 s63, s39, 12\n"
"s_lshl_b32 s34, s61, 10\n"
"s_add_u32 s47, s63, s34\n"
"s_lshl_b32 s34, s57, 3\n"
"s_add_u32 s47, s47, s34\n"
"s_add_u32 s47, s47, 14680064\n"
"s_lshl_b32 s34, s32, 10\n"
"s_add_u32 s42, s63, s34\n"
"s_add_u32 s42, s42, 14680064\n"
"s_add_u32 s43, s63, 14679040\n"
"s_mov_b32 s52, 0x3f803f80\n"
"s_lshl_b32 s53, s32, 8\n"
"s_add_u32 s53, s53, 512\n"
"s_lshl_b32 s62, s61, 16\n"
"s_add_u32 s62, s62, s55\n"
"s_lshl_b32 s34, s57, 8\n"
"s_add_u32 s62, s62, s34\n"
"s_lshl_b32 s34, s32, 6\n"
"s_add_u32 s62, s62, s34\n"
"s_waitcnt lgkmcnt(0)\n"
"s_mov_b32 s8, s92\n"
"s_and_b32 s9, s93, 0xffff\n"
"s_mov_b32 s10, 21561344\n"
"s_mov_b32 s11, 0x20000\n"
"s_mov_b32 s12, s80\n"
"s_and_b32 s13, s81, 0xffff\n"
"s_mov_b32 s14, 2097152\n"
"s_mov_b32 s15, 0x20000\n"
"s_add_u32 m0, s56, 0\n"
"s_nop 0\n"
"buffer_load_dwordx4 v32, s[12:15], s49 offen offset:0 nt lds\n"
"s_mov_b32 s16, s82\n"
"s_and_b32 s17, s83, 0xffff\n"
"s_mov_b32 s18, 1572864\n"
"s_mov_b32 s19, 0x20000\n"
"v_and_b32 v5, 31, v4\n"
"v_lshrrev_b32 v6, 5, v4\n"
"buffer_load_dwordx4 v33, s[12:15], s49 offen offset:1024 nt lds\n"
"v_bfe_u32 v9, v4, 2, 2\n"
"v_and_b32 v10, 3, v4\n"
"v_and_b32 v11, 1, v8\n"
"buffer_load_dwordx4 v34, s[12:15], s49 offen offset:2048 nt lds\n"
"v_lshrrev_b32 v13, 2, v4\n"
"v_mul_u32_u24 v13, 3072, v13\n"
"v_lshlrev_b32 v14, 1, v11\n"
"buffer_load_dwordx4 v35, s[12:15], s49 offen offset:3072 nt lds\n"
"v_xor_b32 v14, v14, v10\n"
"v_lshl_add_u32 v13, v14, 4, v13\n"
"s_add_u32 m0, s56, 4096\n"
"s_add_u32 s78, s49, 32768\n"
"buffer_load_dwordx4 v32, s[12:15], s78 offen offset:0 nt lds\n"
"s_lshl_b32 s34, s58, 6\n"
"v_add_u32 v126, s34, v13\n"
"v_add_u32 v46, 1024, v126\n"
"v_add_u32 v47, 2048, v126\n"
"buffer_load_dwordx4 v33, s[12:15], s78 offen offset:1024 nt lds\n"
"s_mov_b32 s24, s84\n"
"s_and_b32 s25, s85, 0xffff\n"
"s_mov_b32 s26, 524288\n"
"s_mov_b32 s27, 0x20000\n"
"s_mov_b32 s28, s90\n"
"s_and_b32 s29, s91, 0xffff\n"
"s_mov_b32 s30, 2097152\n"
"s_mov_b32 s31, 0x20000\n"
"s_mov_b32 s20, s86\n"
"s_and_b32 s21, s87, 0xffff\n"
"s_mov_b32 s22, s88\n"
"s_and_b32 s23, s89, 0xffff\n"
"buffer_load_dwordx4 v34, s[12:15], s78 offen offset:2048 nt lds\n"
"s_lshl_b32 s34, s39, 4\n"
"s_add_u32 s34, s34, s58\n"
"s_lshl_b32 s36, s34, 10\n"
"s_add_u32 s36, s36, 15728640\n"
"s_cmp_gt_u32 s59, 0\n"
"s_cselect_b32 s74, 0xf0000, 0\n"
"s_or_b32 s74, s74, 0xffff\n"
"s_mov_b32 s75, 65535\n"
"s_cmp_lt_u32 s59, 7\n"
"s_cselect_b32 s76, -1, 0xffff\n"
"s_cmp_ge_u32 s58, 12\n"
"s_cselect_b32 s76, s76, 0xffff\n"
"s_mov_b32 s77, 65535\n"
"buffer_load_dwordx4 v35, s[12:15], s78 offen offset:3072 nt lds\n"
"v_lshlrev_b32 v22, 2, v4\n"
"v_lshlrev_b32 v23, 3, v4\n"
"s_add_u32 m0, s50, 4294868992\n"
"s_add_u32 s78, s79, 48128\n"
"buffer_load_dwordx4 v126, s[16:19], s79 offen offset:0 lds\n"
"buffer_load_dwordx4 v126, s[16:19], s78 offen offset:1024 lds\n"
"s_add_u32 m0, s50, 4294901760\n"
"s_add_u32 s78, s79, 48128\n"
"buffer_load_dwordx4 v46, s[16:19], s79 offen offset:0 lds\n"
"buffer_load_dwordx4 v46, s[16:19], s78 offen offset:1024 lds\n"
"s_bitcmp1_b32 s38, 2\n"
"s_cbranch_scc1 L_spx1\n"
"s_lshr_b32 s4, s38, 3\n"
"s_lshl_b32 s4, s4, 2\n"
"s_and_b32 s5, s38, 3\n"
"s_add_u32 s4, s4, s5\n"
"s_lshl_b32 s4, s4, 2\n"
"s_add_u32 s4, s4, s32\n"
"s_lshl_b32 s34, s4, 10\n"
"s_add_u32 s6, s84, s34\n"
"s_addc_u32 s7, s85, 0\n"
"s_load_dword s3, s[6:7], 0\n"
"s_load_dword s3, s[6:7], 64\n"
"s_load_dword s3, s[6:7], 128\n"
"s_load_dword s3, s[6:7], 192\n"
"s_load_dword s3, s[6:7], 256\n"
"s_load_dword s3, s[6:7], 320\n"
"s_load_dword s3, s[6:7], 384\n"
"s_load_dword s3, s[6:7], 448\n"
"s_load_dword s3, s[6:7], 512\n"
"s_load_dword s3, s[6:7], 576\n"
"s_load_dword s3, s[6:7], 640\n"
"s_load_dword s3, s[6:7], 704\n"
"s_load_dword s3, s[6:7], 768\n"
"s_load_dword s3, s[6:7], 832\n"
"s_load_dword s3, s[6:7], 896\n"
"s_load_dword s3, s[6:7], 960\n"
"L_spx1:\n"
"v_add_u32 v116, 98304, v115\n"
"v_add_u32 v94, 147456, v115\n"
"v_and_b32 v17, 15, v5\n"
"v_xor_b32 v17, v17, v6\n"
"v_lshlrev_b32 v17, 4, v17\n"
"v_lshl_add_u32 v17, v5, 8, v17\n"
"v_add_u32 v36, s56, v17\n"
"v_xor_b32 v37, 32, v36\n"
"v_xor_b32 v38, 64, v36\n"
"v_xor_b32 v39, 96, v36\n"
"v_xor_b32 v40, 128, v36\n"
"v_xor_b32 v41, 160, v36\n"
"v_xor_b32 v42, 192, v36\n"
"v_xor_b32 v43, 224, v36\n"
"v_lshl_add_u32 v17, v6, 3, v9\n"
"v_lshlrev_b32 v17, 6, v17\n"
"v_lshl_add_u32 v17, v10, 3, v17\n"
"v_lshl_add_u32 v104, v11, 5, v17\n"
"v_xor_b32 v18, 1, v11\n"
"v_lshl_add_u32 v17, v18, 5, v17\n"
"v_add_u32 v105, 256, v17\n"
"v_add_u32 v106, 0x10000, v104\n"
"v_add_u32 v107, 0x10000, v105\n"
"s_lshl_b32 s34, s32, 11\n"
"v_add_u32 v44, s34, v104\n"
"v_add_u32 v45, s34, v105\n"
"v_mov_b32 v100, 0\n"
"v_mov_b32 v101, 0\n"
"v_mov_b32 v92, 0\n"
"v_mov_b32 v93, 0\n"
"v_mov_b32 v96, 0\n"
"v_mov_b32 v97, 0\n"
"v_mov_b32 v98, 0\n"
"v_mov_b32 v99, 0\n"
"v_mov_b32 v102, 0\n"
"v_mov_b32 v103, 0\n"
"s_mul_i32 s34, s32, 3072\n"
"v_add_u32 v117, s34, v116\n"
"v_lshl_add_u32 v17, v6, 2, v9\n"
"v_lshlrev_b32 v17, 6, v17\n"
"v_xor_b32 v18, v11, v6\n"
"v_lshl_add_u32 v17, v18, 5, v17\n"
"v_lshl_add_u32 v112, v10, 3, v17\n"
"v_add_u32 v113, 0x10000, v112\n"
"v_lshrrev_b32 v17, 1, v8\n"
"v_lshlrev_b32 v17, 4, v17\n"
"v_lshl_add_u32 v17, v11, 2, v17\n"
"v_add_u32 v17, v17, v9\n"
"v_lshlrev_b32 v17, 6, v17\n"
"v_lshl_add_u32 v17, v10, 3, v17\n"
"v_lshl_add_u32 v19, v8, 3, v9\n"
"v_lshlrev_b32 v19, 6, v19\n"
"v_lshl_add_u32 v19, v10, 3, v19\n"
"s_lshr_b32 s34, s32, 1\n"
"s_add_u32 s34, s34, 1\n"
"s_lshl_b32 s34, s34, 15\n"
"s_and_b32 s35, s32, 1\n"
"v_xor_b32 v18, s35, v11\n"
"v_lshl_add_u32 v17, v18, 5, v17\n"
"v_lshrrev_b32 v17, 1, v8\n"
"v_lshlrev_b32 v17, 10, v17\n"
"v_lshl_add_u32 v17, v11, 9, v17\n"
"v_lshl_add_u32 v17, v7, 4, v17\n"
"v_lshl_add_u32 v17, s35, 8, v17\n"
"v_add_u32 v114, s34, v17\n"
"s_lshr_b32 s34, s32, 1\n"
"s_lshl_b32 s34, s34, 13\n"
"s_add_u32 s34, s34, 32768\n"
"s_lshl_b32 s35, s35, 5\n"
"s_add_u32 s34, s34, s35\n"
"v_add_u32 v108, s34, v19\n"
"s_xor_b32 s34, s34, 32\n"
"s_add_u32 s34, s34, 256\n"
"v_add_u32 v109, s34, v19\n"
"v_and_b32 v17, 7, v7\n"
"v_lshlrev_b32 v18, 4, v17\n"
"v_lshl_add_u32 v18, v8, 7, v18\n"
"v_add_u32 v118, 98304, v18\n"
"v_xor_b32 v18, v8, v17\n"
"v_lshlrev_b32 v18, 4, v18\n"
"v_lshl_add_u32 v18, v17, 10, v18\n"
"v_add_u32 v110, 98304, v18\n"
"v_xor_b32 v111, 64, v110\n"
"v_lshl_add_u32 v17, v11, 5, v7\n"
"v_add_u32 v17, 16, v17\n"
"v_lshlrev_b32 v17, 4, v17\n"
"v_lshrrev_b32 v18, 1, v8\n"
"v_lshl_add_u32 v17, v18, 3, v17\n"
"v_bfe_u32 v18, v7, 2, 1\n"
"v_lshlrev_b32 v18, 5, v18\n"
"v_lshl_add_u32 v18, v8, 2, v18\n"
"v_lshlrev_b32 v18, 4, v18\n"
"v_lshrrev_b32 v19, 3, v7\n"
"v_lshl_add_u32 v19, v19, 2, v10\n"
"v_lshl_add_u32 v18, v19, 1, v18\n"
"s_lshr_b32 s34, s32, 1\n"
"s_lshl_b32 s34, s34, 8\n"
"v_add_u32 v110, s34, v110\n"
"v_add_u32 v111, s34, v111\n"
"s_lshl_b32 s34, s32, 10\n"
"s_add_u32 s34, s34, 131072\n"
"v_add_u32 v17, s34, v17\n"
"s_and_b32 s34, s32, 1\n"
"s_lshl_b32 s34, s34, 8\n"
"s_add_u32 s34, s34, 133120\n"
"v_add_u32 v18, s34, v18\n"
"s_cmp_lt_u32 s32, 2\n"
"s_cselect_b64 vcc, -1, 0\n"
"v_cndmask_b32 v121, v18, v17\n"
"s_lshl_b32 s34, s32, 7\n"
"s_add_u32 s34, s34, 146496\n"
"v_lshl_add_u32 v17, v5, 2, s34\n"
"v_lshlrev_b32 v18, 2, v7\n"
"v_add_u32 v18, 146432, v18\n"
"v_mov_b32 v19, 147008\n"
"v_cmp_gt_u32 vcc, 48, v4\n"
"s_nop 1\n"
"v_cndmask_b32 v18, v19, v18\n"
"v_cmp_gt_u32 vcc, 32, v4\n"
"s_nop 1\n"
"v_cndmask_b32 v127, v18, v17\n"
"v_lshlrev_b32 v18, 2, v6\n"
"v_sub_u32 v20, v5, v18\n"
"v_subrev_u32 v18, 0, v20\n"
"v_and_b32 v18, 7, v18\n"
"v_lshlrev_b32 v18, 2, v18\n"
"v_sub_u32 v122, v17, v18\n"
"v_subrev_u32 v18, 1, v20\n"
"v_and_b32 v18, 7, v18\n"
"v_lshlrev_b32 v18, 2, v18\n"
"v_sub_u32 v123, v17, v18\n"
"v_subrev_u32 v18, 2, v20\n"
"v_and_b32 v18, 7, v18\n"
"v_lshlrev_b32 v18, 2, v18\n"
"v_sub_u32 v124, v17, v18\n"
"v_subrev_u32 v18, 3, v20\n"
"v_and_b32 v18, 7, v18\n"
"v_lshlrev_b32 v18, 2, v18\n"
"v_sub_u32 v125, v17, v18\n"
"v_add_u32 v17, 155648, v115\n"
"v_mov_b32 v18, 147088\n"
"s_or_b32 s34, s59, s32\n"
"s_cmp_eq_u32 s34, 0\n"
"s_cselect_b64 vcc, -1, 0\n"
"v_cndmask_b32 v95, v17, v18\n"
"v_lshrrev_b32 v17, 8, v115\n"
"v_bfe_u32 v119, v115, 4, 4\n"
"v_lshlrev_b32 v18, 2, v17\n"
"v_xor_b32 v119, v119, v18\n"
"v_lshlrev_b32 v119, 4, v119\n"
"v_lshl_add_u32 v119, v17, 10, v119\n"
"s_lshl_b32 s34, s57, 8\n"
"s_lshl_b32 s35, s32, 15\n"
"s_add_u32 s34, s34, s35\n"
"s_add_u32 s34, s34, 0\n"
"v_add_u32 v119, s34, v119\n"
"s_lshr_b32 s34, s39, 2\n"
"s_sub_u32 s34, s34, s57\n"
"s_sub_u32 s34, s34, 1\n"
"s_and_b32 s34, s34, 3\n"
"s_lshl_b32 s34, s34, 3\n"
"s_and_b32 s35, s39, 3\n"
"s_lshl_b32 s35, s35, 1\n"
"s_add_u32 s34, s34, s35\n"
"s_and_b32 s100, s32, 1\n"
"s_add_u32 s34, s34, s100\n"
"s_lshr_b32 s35, s32, 1\n"
"s_lshl_b32 s35, s35, 5\n"
"s_add_u32 s34, s34, s35\n"
"s_lshl_b32 s33, s34, 10\n"
"s_lshl_b32 s54, s39, 1\n"
"s_add_u32 s100, s100, s54\n"
"s_add_u32 s100, s100, s35\n"
"s_lshl_b32 s100, s100, 10\n"
"s_lshl_b32 s35, s58, 17\n"
"s_add_u32 s35, s35, 16777216\n"
"s_add_u32 s100, s100, s35\n"
"s_lshl_b32 s54, s54, 10\n"
"s_add_u32 s54, s54, s35\n"
"s_min_u32 s34, s32, 2\n"
"s_lshl_b32 s34, s34, 15\n"
"s_add_u32 s54, s54, s34\n"
"s_cmp_lt_u32 s32, 3\n"
"s_cselect_b32 s34, 0x200000, 0\n"
"s_add_u32 s54, s54, s34\n"
"v_subrev_u32 v64, 0, v20\n"
"v_subrev_u32 v65, 1, v20\n"
"v_subrev_u32 v66, 2, v20\n"
"v_subrev_u32 v67, 3, v20\n"
"v_ashrrev_i32 v64, 3, v64\n"
"v_ashrrev_i32 v65, 3, v65\n"
"v_ashrrev_i32 v66, 3, v66\n"
"v_ashrrev_i32 v67, 3, v67\n"
"v_mov_b32 v61, 0xff800000\n"
"v_mov_b32 v62, 0\n"
"v_mov_b32 v60, 0xffff\n"
"v_mov_b32 v63, 0xffff0000\n"
"v_cmp_eq_u32 vcc, s32, v64\n"
"s_nop 1\n"
"v_cndmask_b32 v56, v61, v62\n"
"v_cmp_eq_u32 vcc, s32, v65\n"
"s_nop 1\n"
"v_cndmask_b32 v57, v61, v62\n"
"v_cmp_eq_u32 vcc, s32, v66\n"
"s_nop 1\n"
"v_cndmask_b32 v58, v61, v62\n"
"v_cmp_eq_u32 vcc, s32, v67\n"
"s_nop 1\n"
"v_cndmask_b32 v59, v61, v62\n"
"s_lshl_b32 s34, s32, 10\n"
"v_add_u32 v55, s34, v116\n"
"ds_write_b128 v55, v[56:59] offset:53248\n"
"s_lshl_b32 s34, s32, 1\n"
"s_sub_u32 s34, s34, 3\n"
"s_add_u32 s35, s34, 1\n"
"v_cmp_eq_u32 vcc, s34, v64\n"
"s_nop 1\n"
"v_cndmask_b32 v68, v62, v60\n"
"v_cmp_eq_u32 vcc, s34, v65\n"
"s_nop 1\n"
"v_cndmask_b32 v69, v62, v63\n"
"v_cmp_eq_u32 vcc, s34, v66\n"
"s_nop 1\n"
"v_cndmask_b32 v70, v62, v60\n"
"v_cmp_eq_u32 vcc, s34, v67\n"
"s_nop 1\n"
"v_cndmask_b32 v71, v62, v63\n"
"v_cmp_eq_u32 vcc, s35, v64\n"
"s_nop 1\n"
"v_cndmask_b32 v48, v62, v60\n"
"v_cmp_eq_u32 vcc, s35, v65\n"
"s_nop 1\n"
"v_cndmask_b32 v49, v62, v63\n"
"v_cmp_eq_u32 vcc, s35, v66\n"
"s_nop 1\n"
"v_cndmask_b32 v50, v62, v60\n"
"v_cmp_eq_u32 vcc, s35, v67\n"
"s_nop 1\n"
"v_cndmask_b32 v51, v62, v63\n"
"v_or_b32 v56, v68, v69\n"
"v_or_b32 v57, v70, v71\n"
"v_or_b32 v58, v48, v49\n"
"v_or_b32 v59, v50, v51\n"
"s_cmp_lg_u32 s32, 0\n"
"s_cbranch_scc1 L_tb\n"
"v_cmp_eq_u32 vcc, -1, v64\n"
"s_nop 1\n"
"v_cndmask_b32 v56, v61, v62\n"
"v_cmp_eq_u32 vcc, -1, v65\n"
"s_nop 1\n"
"v_cndmask_b32 v57, v61, v62\n"
"v_cmp_eq_u32 vcc, -1, v66\n"
"s_nop 1\n"
"v_cndmask_b32 v58, v61, v62\n"
"v_cmp_eq_u32 vcc, -1, v67\n"
"s_nop 1\n"
"v_cndmask_b32 v59, v61, v62\n"
"L_tb:\n"
"ds_write_b128 v55, v[56:59] offset:57344\n"
"s_bitcmp1_b32 s38, 2\n"
"s_cbranch_scc1 L_spx2\n"
"s_lshr_b32 s4, s38, 3\n"
"s_lshl_b32 s4, s4, 2\n"
"s_and_b32 s5, s38, 3\n"
"s_add_u32 s4, s4, s5\n"
"s_lshl_b32 s4, s4, 2\n"
"s_add_u32 s4, s4, s32\n"
"s_lshl_b32 s34, s4, 10\n"
"s_add_u32 s6, s88, s34\n"
"s_addc_u32 s7, s89, 0\n"
"s_load_dword s3, s[6:7], 0\n"
"s_load_dword s3, s[6:7], 64\n"
"s_load_dword s3, s[6:7], 128\n"
"s_load_dword s3, s[6:7], 192\n"
"s_load_dword s3, s[6:7], 256\n"
"s_load_dword s3, s[6:7], 320\n"
"s_load_dword s3, s[6:7], 384\n"
"s_load_dword s3, s[6:7], 448\n"
"s_load_dword s3, s[6:7], 512\n"
"s_load_dword s3, s[6:7], 576\n"
"s_load_dword s3, s[6:7], 640\n"
"s_load_dword s3, s[6:7], 704\n"
"s_load_dword s3, s[6:7], 768\n"
"s_load_dword s3, s[6:7], 832\n"
"s_load_dword s3, s[6:7], 896\n"
"s_load_dword s3, s[6:7], 960\n"
"L_spx2:\n"
"s_add_u32 s60, s60, 1\n"
"s_and_b32 s60, s60, 3\n"
"s_lshl_b32 s49, s60, 8\n"
"s_add_u32 s49, s49, s55\n"
"s_mul_i32 s79, s60, 393216\n"
"s_mul_i32 s78, s32, 98304\n"
"s_add_u32 s79, s79, s78\n"
"s_add_u32 m0, s56, 49152\n"
"s_nop 0\n"
"buffer_load_dwordx4 v32, s[12:15], s49 offen offset:0 nt lds\n"
"buffer_load_dwordx4 v33, s[12:15], s49 offen offset:1024 nt lds\n"
"buffer_load_dwordx4 v34, s[12:15], s49 offen offset:2048 nt lds\n"
"buffer_load_dwordx4 v35, s[12:15], s49 offen offset:3072 nt lds\n"
"s_add_u32 m0, s56, 53248\n"
"s_add_u32 s78, s49, 32768\n"
"buffer_load_dwordx4 v32, s[12:15], s78 offen offset:0 nt lds\n"
"buffer_load_dwordx4 v33, s[12:15], s78 offen offset:1024 nt lds\n"
"buffer_load_dwordx4 v34, s[12:15], s78 offen offset:2048 nt lds\n"
"buffer_load_dwordx4 v35, s[12:15], s78 offen offset:3072 nt lds\n"
"s_add_u32 m0, s50, 4294877184\n"
"s_add_u32 s78, s79, 48128\n"
"buffer_load_dwordx4 v126, s[16:19], s79 offen offset:0 lds\n"
"buffer_load_dwordx4 v126, s[16:19], s78 offen offset:1024 lds\n"
"s_add_u32 m0, s50, 4294909952\n"
"s_add_u32 s78, s79, 48128\n"
"buffer_load_dwordx4 v46, s[16:19], s79 offen offset:0 lds\n"
"buffer_load_dwordx4 v46, s[16:19], s78 offen offset:1024 lds\n"
"s_cmp_lg_u32 s32, 0\n"
"s_cbranch_scc1 L_hp0\n"
"v_cmp_gt_u32 vcc, 256, v115\n"
"s_lshl_b32 s34, s39, 14\n"
"s_add_u32 s34, s34, 15728640\n"
"s_lshl_b32 s35, s58, 3\n"
"s_add_u32 s34, s34, s35\n"
"v_lshl_add_u32 v240, v115, 6, s34\n"
"v_add_u32 v242, 512, v240\n"
"v_add_u32 v241, -19840, v240\n"
"v_cndmask_b32 v242, v241, v242\n"
"v_add_u32 v241, 32, v240\n"
"v_cndmask_b32 v243, v241, v240\n"
"s_lshl_b32 s34, s58, 10\n"
"s_lshl_b32 s35, s39, 3\n"
"s_add_u32 s34, s34, s35\n"
"s_add_u32 s34, s34, 15204608\n"
"v_cmp_gt_u32 vcc, 512, v115\n"
"v_lshl_add_u32 v240, v115, 10, s34\n"
"v_add_u32 v241, 512, v240\n"
"v_cndmask_b32 v243, v240, v243\n"
"v_cndmask_b32 v242, v241, v242\n"
"v_accvgpr_write_b32 a130, v242\n"
"v_accvgpr_write_b32 a131, v243\n"
"s_mov_b64 exec, s[74:75]\n"
"buffer_load_dword v244, v242, s[8:11], 0 offen sc1\n"
"s_mov_b64 exec, -1\n"
"L_hp0:\n"
"s_waitcnt vmcnt(12)\n"
"ds_read_b128 v[128:131], v36 offset:0\n"
"ds_read_b128 v[132:135], v37 offset:0\n"
"ds_read_b128 v[136:139], v38 offset:0\n"
"ds_read_b128 v[140:143], v39 offset:0\n"
"ds_read_b128 v[144:147], v40 offset:0\n"
"ds_read_b128 v[148:151], v41 offset:0\n"
"ds_read_b128 v[152:155], v42 offset:0\n"
"ds_read_b128 v[156:159], v43 offset:0\n"
"s_waitcnt lgkmcnt(0)\n"
"s_barrier\n"
"ds_read_b64_tr_b16 v[72:73], v44 offset:0\n"
"ds_read_b64_tr_b16 v[74:75], v45 offset:0\n"
"ds_read_b64_tr_b16 v[76:77], v44 offset:1024\n"
"ds_read_b64_tr_b16 v[78:79], v45 offset:1024\n"
"ds_read_b64_tr_b16 v[80:81], v44 offset:32768\n"
"ds_read_b64_tr_b16 v[82:83], v45 offset:32768\n"
"ds_read_b64_tr_b16 v[84:85], v44 offset:33792\n"
"ds_read_b64_tr_b16 v[86:87], v45 offset:33792\n"
"v_dot2c_f32_bf16 v92, s52, v128\n"
"v_dot2c_f32_bf16 v93, v128, v128\n"
"v_dot2c_f32_bf16 v100, s52, v129\n"
"v_dot2c_f32_bf16 v101, v129, v129\n"
"v_dot2c_f32_bf16 v92, s52, v130\n"
"v_dot2c_f32_bf16 v93, v130, v130\n"
"v_dot2c_f32_bf16 v100, s52, v131\n"
"v_dot2c_f32_bf16 v101, v131, v131\n"
"s_add_u32 s60, s60, 1\n"
"s_and_b32 s60, s60, 3\n"
"s_lshl_b32 s49, s60, 8\n"
"s_add_u32 s49, s49, s55\n"
"s_mul_i32 s79, s60, 393216\n"
"s_mul_i32 s78, s32, 98304\n"
"s_add_u32 s79, s79, s78\n"
"v_dot2c_f32_bf16 v92, s52, v132\n"
"v_dot2c_f32_bf16 v93, v132, v132\n"
"v_dot2c_f32_bf16 v100, s52, v133\n"
"v_dot2c_f32_bf16 v101, v133, v133\n"
"v_dot2c_f32_bf16 v92, s52, v134\n"
"v_dot2c_f32_bf16 v93, v134, v134\n"
"v_dot2c_f32_bf16 v100, s52, v135\n"
"v_dot2c_f32_bf16 v101, v135, v135\n"
"ds_read_b64_tr_b16 v[48:49], v104 offset:0\n"
"ds_read_b64_tr_b16 v[50:51], v105 offset:0\n"
"ds_read_b64_tr_b16 v[52:53], v104 offset:32768\n"
"ds_read_b64_tr_b16 v[54:55], v105 offset:32768\n"
"s_add_u32 m0, s56, 0\n"
"s_nop 0\n"
"buffer_load_dwordx4 v32, s[12:15], s49 offen offset:0 nt lds\n"
"v_dot2c_f32_bf16 v92, s52, v136\n"
"v_dot2c_f32_bf16 v93, v136, v136\n"
"v_dot2c_f32_bf16 v100, s52, v137\n"
"v_dot2c_f32_bf16 v101, v137, v137\n"
"v_dot2c_f32_bf16 v92, s52, v138\n"
"v_dot2c_f32_bf16 v93, v138, v138\n"
"v_dot2c_f32_bf16 v100, s52, v139\n"
"v_dot2c_f32_bf16 v101, v139, v139\n"
"ds_read_b64_tr_b16 v[56:57], v104 offset:1024\n"
"ds_read_b64_tr_b16 v[58:59], v105 offset:1024\n"
"ds_read_b64_tr_b16 v[60:61], v104 offset:33792\n"
"ds_read_b64_tr_b16 v[62:63], v105 offset:33792\n"
"v_dot2c_f32_bf16 v92, s52, v140\n"
"v_dot2c_f32_bf16 v93, v140, v140\n"
"v_dot2c_f32_bf16 v100, s52, v141\n"
"v_dot2c_f32_bf16 v101, v141, v141\n"
"v_dot2c_f32_bf16 v92, s52, v142\n"
"v_dot2c_f32_bf16 v93, v142, v142\n"
"v_dot2c_f32_bf16 v100, s52, v143\n"
"v_dot2c_f32_bf16 v101, v143, v143\n"
"buffer_load_dwordx4 v33, s[12:15], s49 offen offset:1024 nt lds\n"
"v_dot2c_f32_bf16 v92, s52, v144\n"
"v_dot2c_f32_bf16 v93, v144, v144\n"
"v_dot2c_f32_bf16 v100, s52, v145\n"
"v_dot2c_f32_bf16 v101, v145, v145\n"
"v_dot2c_f32_bf16 v92, s52, v146\n"
"v_dot2c_f32_bf16 v93, v146, v146\n"
"v_dot2c_f32_bf16 v100, s52, v147\n"
"v_dot2c_f32_bf16 v101, v147, v147\n"
"v_dot2c_f32_bf16 v92, s52, v148\n"
"v_dot2c_f32_bf16 v93, v148, v148\n"
"v_dot2c_f32_bf16 v100, s52, v149\n"
"v_dot2c_f32_bf16 v101, v149, v149\n"
"v_dot2c_f32_bf16 v92, s52, v150\n"
"v_dot2c_f32_bf16 v93, v150, v150\n"
"v_dot2c_f32_bf16 v100, s52, v151\n"
"v_dot2c_f32_bf16 v101, v151, v151\n"
"buffer_load_dwordx4 v34, s[12:15], s49 offen offset:2048 nt lds\n"
"v_dot2c_f32_bf16 v92, s52, v152\n"
"v_dot2c_f32_bf16 v93, v152, v152\n"
"s_waitcnt lgkmcnt(15)\n"
"v_dot2c_f32_bf16 v96, s52, v72\n"
"s_waitcnt lgkmcnt(11)\n"
"v_dot2c_f32_bf16 v97, s52, v80\n"
"v_dot2c_f32_bf16 v100, s52, v153\n"
"v_dot2c_f32_bf16 v101, v153, v153\n"
"v_dot2c_f32_bf16 v96, s52, v73\n"
"v_dot2c_f32_bf16 v97, s52, v81\n"
"v_dot2c_f32_bf16 v92, s52, v154\n"
"v_dot2c_f32_bf16 v93, v154, v154\n"
"v_dot2c_f32_bf16 v96, s52, v74\n"
"s_waitcnt lgkmcnt(10)\n"
"v_dot2c_f32_bf16 v97, s52, v82\n"
"v_dot2c_f32_bf16 v100, s52, v155\n"
"v_dot2c_f32_bf16 v101, v155, v155\n"
"v_dot2c_f32_bf16 v96, s52, v75\n"
"v_dot2c_f32_bf16 v97, s52, v83\n"
"buffer_load_dwordx4 v35, s[12:15], s49 offen offset:3072 nt lds\n"
"v_dot2c_f32_bf16 v92, s52, v156\n"
"v_dot2c_f32_bf16 v93, v156, v156\n"
"v_dot2c_f32_bf16 v96, s52, v76\n"
"s_waitcnt lgkmcnt(9)\n"
"v_dot2c_f32_bf16 v97, s52, v84\n"
"v_dot2c_f32_bf16 v100, s52, v157\n"
"v_dot2c_f32_bf16 v101, v157, v157\n"
"v_dot2c_f32_bf16 v96, s52, v77\n"
"v_dot2c_f32_bf16 v97, s52, v85\n"
"v_dot2c_f32_bf16 v92, s52, v158\n"
"v_dot2c_f32_bf16 v93, v158, v158\n"
"v_dot2c_f32_bf16 v96, s52, v78\n"
"s_waitcnt lgkmcnt(8)\n"
"v_dot2c_f32_bf16 v97, s52, v86\n"
"v_dot2c_f32_bf16 v100, s52, v159\n"
"v_dot2c_f32_bf16 v101, v159, v159\n"
"v_dot2c_f32_bf16 v96, s52, v79\n"
"v_dot2c_f32_bf16 v97, s52, v87\n"
"s_add_u32 m0, s56, 4096\n"
"s_add_u32 s78, s49, 32768\n"
"buffer_load_dwordx4 v32, s[12:15], s78 offen offset:0 nt lds\n"
"s_waitcnt lgkmcnt(6)\n"
"v_mfma_f32_32x32x16_bf16 v[0:15], v[48:51], v[128:131], 0\n"
"ds_read_b64_tr_b16 v[64:65], v104 offset:2048\n"
"ds_read_b64_tr_b16 v[66:67], v105 offset:2048\n"
"s_waitcnt lgkmcnt(6)\n"
"v_mfma_f32_32x32x16_bf16 v[16:31], v[52:55], v[128:131], 0\n"
"ds_read_b64_tr_b16 v[68:69], v104 offset:34816\n"
"ds_read_b64_tr_b16 v[70:71], v105 offset:34816\n"
"buffer_load_dwordx4 v33, s[12:15], s78 offen offset:1024 nt lds\n"
"s_waitcnt lgkmcnt(6)\n"
"v_mfma_f32_32x32x16_bf16 v[0:15], v[56:59], v[132:135], v[0:15]\n"
"ds_read_b64_tr_b16 v[48:49], v104 offset:3072\n"
"ds_read_b64_tr_b16 v[50:51], v105 offset:3072\n"
"s_waitcnt lgkmcnt(6)\n"
"v_mfma_f32_32x32x16_bf16 v[16:31], v[60:63], v[132:135], v[16:31]\n"
"ds_read_b64_tr_b16 v[52:53], v104 offset:35840\n"
"ds_read_b64_tr_b16 v[54:55], v105 offset:35840\n"
"buffer_load_dwordx4 v34, s[12:15], s78 offen offset:2048 nt lds\n"
"s_waitcnt lgkmcnt(6)\n"
"v_mfma_f32_32x32x16_bf16 v[0:15], v[64:67], v[136:139], v[0:15]\n"
"ds_read_b64_tr_b16 v[56:57], v104 offset:4096\n"
"ds_read_b64_tr_b16 v[58:59], v105 offset:4096\n"
"s_waitcnt lgkmcnt(6)\n"
"v_mfma_f32_32x32x16_bf16 v[16:31], v[68:71], v[136:139], v[16:31]\n"
"ds_read_b64_tr_b16 v[60:61], v104 offset:36864\n"
"ds_read_b64_tr_b16 v[62:63], v105 offset:36864\n"
"buffer_load_dwordx4 v35, s[12:15], s78 offen offset:3072 nt lds\n"
"s_waitcnt lgkmcnt(6)\n"
"v_mfma_f32_32x32x16_bf16 v[0:15], v[48:51], v[140:143], v[0:15]\n"
"ds_read_b64_tr_b16 v[64:65], v104 offset:5120\n"
"ds_read_b64_tr_b16 v[66:67], v105 offset:5120\n"
"s_waitcnt lgkmcnt(6)\n"
"v_mfma_f32_32x32x16_bf16 v[16:31], v[52:55], v[140:143], v[16:31]\n"
"ds_read_b64_tr_b16 v[68:69], v104 offset:37888\n"
"ds_read_b64_tr_b16 v[70:71], v105 offset:37888\n"
"s_add_u32 m0, s50, 4294885376\n"
"s_add_u32 s78, s79, 48128\n"
"buffer_load_dwordx4 v126, s[16:19], s79 offen offset:0 lds\n"
"s_waitcnt lgkmcnt(6)\n"
"v_mfma_f32_32x32x16_bf16 v[0:15], v[56:59], v[144:147], v[0:15]\n"
"ds_read_b64_tr_b16 v[48:49], v104 offset:6144\n"
"ds_read_b64_tr_b16 v[50:51], v105 offset:6144\n"
"s_waitcnt lgkmcnt(6)\n"
"v_mfma_f32_32x32x16_bf16 v[16:31], v[60:63], v[144:147], v[16:31]\n"
"ds_read_b64_tr_b16 v[52:53], v104 offset:38912\n"
"ds_read_b64_tr_b16 v[54:55], v105 offset:38912\n"
"buffer_load_dwordx4 v126, s[16:19], s78 offen offset:1024 lds\n"
"s_waitcnt lgkmcnt(6)\n"
"v_mfma_f32_32x32x16_bf16 v[0:15], v[64:67], v[148:151], v[0:15]\n"
"ds_read_b64_tr_b16 v[56:57], v104 offset:7168\n"
"ds_read_b64_tr_b16 v[58:59], v105 offset:7168\n"
"s_waitcnt lgkmcnt(6)\n"
"v_mfma_f32_32x32x16_bf16 v[16:31], v[68:71], v[148:151], v[16:31]\n"
"ds_read_b64_tr_b16 v[60:61], v104 offset:39936\n"
"ds_read_b64_tr_b16 v[62:63], v105 offset:39936\n"
"s_add_u32 m0, s50, 4294918144\n"
"s_add_u32 s78, s79, 48128\n"
"buffer_load_dwordx4 v46, s[16:19], s79 offen offset:0 lds\n"
"s_waitcnt lgkmcnt(6)\n"
"v_mfma_f32_32x32x16_bf16 v[0:15], v[48:51], v[152:155], v[0:15]\n"
"s_waitcnt lgkmcnt(4)\n"
"v_mfma_f32_32x32x16_bf16 v[16:31], v[52:55], v[152:155], v[16:31]\n"
"buffer_load_dwordx4 v46, s[16:19], s78 offen offset:1024 lds\n"
"s_waitcnt lgkmcnt(2)\n"
"v_mfma_f32_32x32x16_bf16 v[0:15], v[56:59], v[156:159], v[0:15]\n"
"s_waitcnt lgkmcnt(0)\n"
"v_mfma_f32_32x32x16_bf16 v[16:31], v[60:63], v[156:159], v[16:31]\n"
"s_waitcnt vmcnt(12)\n"
"ds_read_b128 v[160:163], v36 offset:49152\n"
"ds_read_b128 v[164:167], v37 offset:49152\n"
"ds_read_b128 v[168:171], v38 offset:49152\n"
"ds_read_b128 v[172:175], v39 offset:49152\n"
"ds_read_b128 v[176:179], v40 offset:49152\n"
"ds_read_b128 v[180:183], v41 offset:49152\n"
"ds_read_b128 v[184:187], v42 offset:49152\n"
"ds_read_b128 v[188:191], v43 offset:49152\n"
"s_waitcnt lgkmcnt(0)\n"
"s_barrier\n"
"ds_read_b64_tr_b16 v[72:73], v44 offset:8192\n"
"ds_read_b64_tr_b16 v[74:75], v45 offset:8192\n"
"ds_read_b64_tr_b16 v[76:77], v44 offset:9216\n"
"ds_read_b64_tr_b16 v[78:79], v45 offset:9216\n"
"ds_read_b64_tr_b16 v[80:81], v44 offset:40960\n"
"ds_read_b64_tr_b16 v[82:83], v45 offset:40960\n"
"ds_read_b64_tr_b16 v[84:85], v44 offset:41984\n"
"ds_read_b64_tr_b16 v[86:87], v45 offset:41984\n"
"v_dot2c_f32_bf16 v92, s52, v160\n"
"v_dot2c_f32_bf16 v93, v160, v160\n"
"v_dot2c_f32_bf16 v100, s52, v161\n"
"v_dot2c_f32_bf16 v101, v161, v161\n"
"v_dot2c_f32_bf16 v92, s52, v162\n"
"v_dot2c_f32_bf16 v93, v162, v162\n"
"v_dot2c_f32_bf16 v100, s52, v163\n"
"v_dot2c_f32_bf16 v101, v163, v163\n"
"s_lshl_b32 s34, s32, 5\n"
"s_add_u32 s35, s57, 1\n"
"s_lshl_b32 s35, s35, 8\n"
"v_xor_b32 v88, s34, v115\n"
"v_xor_b32 v89, 16, v88\n"
"v_add_u32 v88, s35, v88\n"
"v_add_u32 v89, s35, v89\n"
"v_and_b32 v88, 1023, v88\n"
"v_and_b32 v89, 1023, v89\n"
"s_cmp_gt_u32 s59, 0\n"
"s_cselect_b32 s34, 0, 1073741824\n"
"v_or_b32 v88, s34, v88\n"
"v_or_b32 v89, s34, v89\n"
"v_dot2c_f32_bf16 v92, s52, v164\n"
"v_dot2c_f32_bf16 v93, v164, v164\n"
"v_dot2c_f32_bf16 v100, s52, v165\n"
"v_dot2c_f32_bf16 v101, v165, v165\n"
"v_dot2c_f32_bf16 v92, s52, v166\n"
"v_dot2c_f32_bf16 v93, v166, v166\n"
"v_dot2c_f32_bf16 v100, s52, v167\n"
"v_dot2c_f32_bf16 v101, v167, v167\n"
"ds_read_b64_tr_b16 v[64:65], v104 offset:8192\n"
"ds_read_b64_tr_b16 v[66:67], v105 offset:8192\n"
"ds_read_b64_tr_b16 v[68:69], v104 offset:40960\n"
"ds_read_b64_tr_b16 v[70:71], v105 offset:40960\n"
"s_max_u32 s34, s55, 16384\n"
"s_sub_u32 s34, s34, 16384\n"
"s_lshl_b32 s35, s32, 12\n"
"s_add_u32 s34, s34, s35\n"
"s_add_u32 s35, s34, 1024\n"
"s_mov_b32 m0, s50\n"
"s_nop 0\n"
"buffer_load_dwordx4 v88, s[12:15], s34 offen offset:0 lds\n"
"buffer_load_dwordx4 v89, s[12:15], s35 offen offset:1024 lds\n"
"v_dot2c_f32_bf16 v92, s52, v168\n"
"v_dot2c_f32_bf16 v93, v168, v168\n"
"v_dot2c_f32_bf16 v100, s52, v169\n"
"v_dot2c_f32_bf16 v101, v169, v169\n"
"v_dot2c_f32_bf16 v92, s52, v170\n"
"v_dot2c_f32_bf16 v93, v170, v170\n"
"v_dot2c_f32_bf16 v100, s52, v171\n"
"v_dot2c_f32_bf16 v101, v171, v171\n"
"ds_read_b64_tr_b16 v[48:49], v104 offset:9216\n"
"ds_read_b64_tr_b16 v[50:51], v105 offset:9216\n"
"ds_read_b64_tr_b16 v[52:53], v104 offset:41984\n"
"ds_read_b64_tr_b16 v[54:55], v105 offset:41984\n"
"v_dot2c_f32_bf16 v92, s52, v172\n"
"v_dot2c_f32_bf16 v93, v172, v172\n"
"v_dot2c_f32_bf16 v100, s52, v173\n"
"v_dot2c_f32_bf16 v101, v173, v173\n"
"v_dot2c_f32_bf16 v92, s52, v174\n"
"v_dot2c_f32_bf16 v93, v174, v174\n"
"v_dot2c_f32_bf16 v100, s52, v175\n"
"v_dot2c_f32_bf16 v101, v175, v175\n"
"s_add_u32 s60, s60, 1\n"
"s_and_b32 s60, s60, 3\n"
"s_lshl_b32 s49, s60, 8\n"
"s_add_u32 s49, s49, s55\n"
"s_mul_i32 s79, s60, 393216\n"
"s_mul_i32 s78, s32, 98304\n"
"s_add_u32 s79, s79, s78\n"
"v_dot2c_f32_bf16 v92, s52, v176\n"
"v_dot2c_f32_bf16 v93, v176, v176\n"
"v_dot2c_f32_bf16 v100, s52, v177\n"
"v_dot2c_f32_bf16 v101, v177, v177\n"
"v_dot2c_f32_bf16 v92, s52, v178\n"
"v_dot2c_f32_bf16 v93, v178, v178\n"
"v_dot2c_f32_bf16 v100, s52, v179\n"
"v_dot2c_f32_bf16 v101, v179, v179\n"
"s_add_u32 m0, s50, 4294893568\n"
"s_add_u32 s78, s79, 48128\n"
"buffer_load_dwordx4 v126, s[16:19], s79 offen offset:0 lds\n"
"v_dot2c_f32_bf16 v92, s52, v180\n"
"v_dot2c_f32_bf16 v93, v180, v180\n"
"v_dot2c_f32_bf16 v100, s52, v181\n"
"v_dot2c_f32_bf16 v101, v181, v181\n"
"v_dot2c_f32_bf16 v92, s52, v182\n"
"v_dot2c_f32_bf16 v93, v182, v182\n"
"v_dot2c_f32_bf16 v100, s52, v183\n"
"v_dot2c_f32_bf16 v101, v183, v183\n"
"v_dot2c_f32_bf16 v92, s52, v184\n"
"v_dot2c_f32_bf16 v93, v184, v184\n"
"s_waitcnt lgkmcnt(15)\n"
"v_dot2c_f32_bf16 v96, s52, v72\n"
"s_waitcnt lgkmcnt(11)\n"
"v_dot2c_f32_bf16 v97, s52, v80\n"
"v_dot2c_f32_bf16 v100, s52, v185\n"
"v_dot2c_f32_bf16 v101, v185, v185\n"
"v_dot2c_f32_bf16 v96, s52, v73\n"
"v_dot2c_f32_bf16 v97, s52, v81\n"
"buffer_load_dwordx4 v126, s[16:19], s78 offen offset:1024 lds\n"
"v_dot2c_f32_bf16 v92, s52, v186\n"
"v_dot2c_f32_bf16 v93, v186, v186\n"
"v_dot2c_f32_bf16 v96, s52, v74\n"
"s_waitcnt lgkmcnt(10)\n"
"v_dot2c_f32_bf16 v97, s52, v82\n"
"v_dot2c_f32_bf16 v100, s52, v187\n"
"v_dot2c_f32_bf16 v101, v187, v187\n"
"v_dot2c_f32_bf16 v96, s52, v75\n"
"v_dot2c_f32_bf16 v97, s52, v83\n"
"s_add_u32 m0, s50, 4294926336\n"
"s_add_u32 s78, s79, 48128\n"
"buffer_load_dwordx4 v46, s[16:19], s79 offen offset:0 lds\n"
"v_dot2c_f32_bf16 v92, s52, v188\n"
"v_dot2c_f32_bf16 v93, v188, v188\n"
"v_dot2c_f32_bf16 v96, s52, v76\n"
"s_waitcnt lgkmcnt(9)\n"
"v_dot2c_f32_bf16 v97, s52, v84\n"
"v_dot2c_f32_bf16 v100, s52, v189\n"
"v_dot2c_f32_bf16 v101, v189, v189\n"
"v_dot2c_f32_bf16 v96, s52, v77\n"
"v_dot2c_f32_bf16 v97, s52, v85\n"
"v_dot2c_f32_bf16 v92, s52, v190\n"
"v_dot2c_f32_bf16 v93, v190, v190\n"
"v_dot2c_f32_bf16 v96, s52, v78\n"
"s_waitcnt lgkmcnt(8)\n"
"v_dot2c_f32_bf16 v97, s52, v86\n"
"v_dot2c_f32_bf16 v100, s52, v191\n"
"v_dot2c_f32_bf16 v101, v191, v191\n"
"v_dot2c_f32_bf16 v96, s52, v79\n"
"v_dot2c_f32_bf16 v97, s52, v87\n"
"buffer_load_dwordx4 v46, s[16:19], s78 offen offset:1024 lds\n"
"s_waitcnt lgkmcnt(6)\n"
"v_mfma_f32_32x32x16_bf16 v[0:15], v[64:67], v[160:163], v[0:15]\n"
"ds_read_b64_tr_b16 v[56:57], v104 offset:10240\n"
"ds_read_b64_tr_b16 v[58:59], v105 offset:10240\n"
"s_add_u32 m0, s56, 49152\n"
"s_nop 0\n"
"buffer_load_dwordx4 v32, s[12:15], s49 offen offset:0 nt lds\n"
"s_waitcnt lgkmcnt(6)\n"
"v_mfma_f32_32x32x16_bf16 v[16:31], v[68:71], v[160:163], v[16:31]\n"
"ds_read_b64_tr_b16 v[60:61], v104 offset:43008\n"
"ds_read_b64_tr_b16 v[62:63], v105 offset:43008\n"
"s_waitcnt lgkmcnt(6)\n"
"v_mfma_f32_32x32x16_bf16 v[0:15], v[48:51], v[164:167], v[0:15]\n"
"ds_read_b64_tr_b16 v[64:65], v104 offset:11264\n"
"ds_read_b64_tr_b16 v[66:67], v105 offset:11264\n"
"buffer_load_dwordx4 v33, s[12:15], s49 offen offset:1024 nt lds\n"
"s_waitcnt lgkmcnt(6)\n"
"v_mfma_f32_32x32x16_bf16 v[16:31], v[52:55], v[164:167], v[16:31]\n"
"ds_read_b64_tr_b16 v[68:69], v104 offset:44032\n"
"ds_read_b64_tr_b16 v[70:71], v105 offset:44032\n"
"s_waitcnt lgkmcnt(6)\n"
"v_mfma_f32_32x32x16_bf16 v[0:15], v[56:59], v[168:171], v[0:15]\n"
"ds_read_b64_tr_b16 v[48:49], v104 offset:12288\n"
"ds_read_b64_tr_b16 v[50:51], v105 offset:12288\n"
"buffer_load_dwordx4 v34, s[12:15], s49 offen offset:2048 nt lds\n"
"s_waitcnt lgkmcnt(6)\n"
"v_mfma_f32_32x32x16_bf16 v[16:31], v[60:63], v[168:171], v[16:31]\n"
"ds_read_b64_tr_b16 v[52:53], v104 offset:45056\n"
"ds_read_b64_tr_b16 v[54:55], v105 offset:45056\n"
"buffer_load_dwordx4 v35, s[12:15], s49 offen offset:3072 nt lds\n"
"s_waitcnt lgkmcnt(6)\n"
"v_mfma_f32_32x32x16_bf16 v[0:15], v[64:67], v[172:175], v[0:15]\n"
"ds_read_b64_tr_b16 v[56:57], v104 offset:13312\n"
"ds_read_b64_tr_b16 v[58:59], v105 offset:13312\n"
"s_waitcnt lgkmcnt(6)\n"
"v_mfma_f32_32x32x16_bf16 v[16:31], v[68:71], v[172:175], v[16:31]\n"
"ds_read_b64_tr_b16 v[60:61], v104 offset:46080\n"
"ds_read_b64_tr_b16 v[62:63], v105 offset:46080\n"
"s_add_u32 m0, s56, 53248\n"
"s_add_u32 s78, s49, 32768\n"
"buffer_load_dwordx4 v32, s[12:15], s78 offen offset:0 nt lds\n"
"s_waitcnt lgkmcnt(6)\n"
"v_mfma_f32_32x32x16_bf16 v[0:15], v[48:51], v[176:179], v[0:15]\n"
"ds_read_b64_tr_b16 v[64:65], v104 offset:14336\n"
"ds_read_b64_tr_b16 v[66:67], v105 offset:14336\n"
"buffer_load_dwordx4 v33, s[12:15], s78 offen offset:1024 nt lds\n"
"s_waitcnt lgkmcnt(6)\n"
"v_mfma_f32_32x32x16_bf16 v[16:31], v[52:55], v[176:179], v[16:31]\n"
"ds_read_b64_tr_b16 v[68:69], v104 offset:47104\n"
"ds_read_b64_tr_b16 v[70:71], v105 offset:47104\n"
"s_waitcnt lgkmcnt(6)\n"
"v_mfma_f32_32x32x16_bf16 v[0:15], v[56:59], v[180:183], v[0:15]\n"
"ds_read_b64_tr_b16 v[48:49], v104 offset:15360\n"
"ds_read_b64_tr_b16 v[50:51], v105 offset:15360\n"
"buffer_load_dwordx4 v34, s[12:15], s78 offen offset:2048 nt lds\n"
"s_waitcnt lgkmcnt(6)\n"
"v_mfma_f32_32x32x16_bf16 v[16:31], v[60:63], v[180:183], v[16:31]\n"
"ds_read_b64_tr_b16 v[52:53], v104 offset:48128\n"
"ds_read_b64_tr_b16 v[54:55], v105 offset:48128\n"
"buffer_load_dwordx4 v35, s[12:15], s78 offen offset:3072 nt lds\n"
"s_waitcnt lgkmcnt(6)\n"
"v_mfma_f32_32x32x16_bf16 v[0:15], v[64:67], v[184:187], v[0:15]\n"
"s_waitcnt lgkmcnt(4)\n"
"v_mfma_f32_32x32x16_bf16 v[16:31], v[68:71], v[184:187], v[16:31]\n"
"s_cmp_lg_u32 s32, 0\n"
"s_cbranch_scc1 L_hp1\n"
"v_lshrrev_b32 v240, 1, v115\n"
"v_add_u32 v244, 1, v244\n"
"s_mov_b64 exec, s[74:75]\n"
"buffer_store_dword v244, v240, s[8:11], s36 offen sc1\n"
"s_mov_b64 exec, -1\n"
"v_accvgpr_write_b32 a128, v244\n"
"L_hp1:\n"
"s_waitcnt lgkmcnt(2)\n"
"v_mfma_f32_32x32x16_bf16 v[0:15], v[48:51], v[188:191], v[0:15]\n"
"s_waitcnt lgkmcnt(0)\n"
"v_mfma_f32_32x32x16_bf16 v[16:31], v[52:55], v[188:191], v[16:31]\n"
"s_waitcnt vmcnt(12)\n"
"ds_read_b128 v[192:195], v36 offset:0\n"
"ds_read_b128 v[196:199], v37 offset:0\n"
"ds_read_b128 v[200:203], v38 offset:0\n"
"ds_read_b128 v[204:207], v39 offset:0\n"
"ds_read_b128 v[208:211], v40 offset:0\n"
"ds_read_b128 v[212:215], v41 offset:0\n"
"ds_read_b128 v[216:219], v42 offset:0\n"
"ds_read_b128 v[220:223], v43 offset:0\n"
"s_waitcnt lgkmcnt(0)\n"
"s_barrier\n"
"ds_read_b64_tr_b16 v[72:73], v44 offset:16384\n"
"ds_read_b64_tr_b16 v[74:75], v45 offset:16384\n"
"ds_read_b64_tr_b16 v[76:77], v44 offset:17408\n"
"ds_read_b64_tr_b16 v[78:79], v45 offset:17408\n"
"ds_read_b64_tr_b16 v[80:81], v44 offset:49152\n"
"ds_read_b64_tr_b16 v[82:83], v45 offset:49152\n"
"ds_read_b64_tr_b16 v[84:85], v44 offset:50176\n"
"ds_read_b64_tr_b16 v[86:87], v45 offset:50176\n"
"s_lshr_b32 s34, s32, 1\n"
"s_sub_u32 s35, s32, s34\n"
"s_lshl_b32 s34, s35, 8\n"
"v_add_u32 v88, s34, v110\n"
"v_add_u32 v89, s34, v111\n"
"v_mov_b32 v34, 0\n"
"v_mov_b32 v35, 0\n"
"ds_read_b128 v[224:227], v88 offset:0\n"
"ds_read_b128 v[228:231], v89 offset:0\n"
"ds_read_b128 v[232:235], v88 offset:128\n"
"ds_read_b128 v[236:239], v89 offset:128\n"
"v_dot2c_f32_bf16 v92, s52, v192\n"
"v_dot2c_f32_bf16 v93, v192, v192\n"
"v_dot2c_f32_bf16 v100, s52, v193\n"
"v_dot2c_f32_bf16 v101, v193, v193\n"
"v_dot2c_f32_bf16 v92, s52, v194\n"
"v_dot2c_f32_bf16 v93, v194, v194\n"
"v_dot2c_f32_bf16 v100, s52, v195\n"
"v_dot2c_f32_bf16 v101, v195, v195\n"
"s_cmp_lt_u32 s32, 3\n"
"s_cselect_b32 s4, s20, s16\n"
"s_cselect_b32 s5, s21, s17\n"
"s_min_u32 s34, s32, 2\n"
"s_lshl_b32 s34, s34, 10\n"
"s_mul_i32 s35, s39, 98304\n"
"s_add_u32 s34, s34, s35\n"
"s_add_u32 s3, s34, 48128\n"
"s_mov_b32 s6, 1572864\n"
"s_mov_b32 s7, 0x20000\n"
"s_add_u32 m0, s50, 8192\n"
"s_nop 0\n"
"buffer_load_dwordx4 v126, s[4:7], s34 offen offset:0 lds\n"
"buffer_load_dwordx4 v126, s[4:7], s3 offen offset:1024 lds\n"
"v_dot2c_f32_bf16 v92, s52, v196\n"
"v_dot2c_f32_bf16 v93, v196, v196\n"
"v_dot2c_f32_bf16 v100, s52, v197\n"
"v_dot2c_f32_bf16 v101, v197, v197\n"
"v_dot2c_f32_bf16 v92, s52, v198\n"
"v_dot2c_f32_bf16 v93, v198, v198\n"
"v_dot2c_f32_bf16 v100, s52, v199\n"
"v_dot2c_f32_bf16 v101, v199, v199\n"
"ds_read_b64_tr_b16 v[56:57], v104 offset:16384\n"
"ds_read_b64_tr_b16 v[58:59], v105 offset:16384\n"
"ds_read_b64_tr_b16 v[60:61], v104 offset:49152\n"
"ds_read_b64_tr_b16 v[62:63], v105 offset:49152\n"
"v_dot2c_f32_bf16 v92, s52, v200\n"
"v_dot2c_f32_bf16 v93, v200, v200\n"
"s_waitcnt lgkmcnt(15)\n"
"v_dot2c_f32_bf16 v96, s52, v72\n"
"s_waitcnt lgkmcnt(11)\n"
"v_dot2c_f32_bf16 v97, s52, v80\n"
"v_dot2c_f32_bf16 v100, s52, v201\n"
"v_dot2c_f32_bf16 v101, v201, v201\n"
"v_dot2c_f32_bf16 v96, s52, v73\n"
"v_dot2c_f32_bf16 v97, s52, v81\n"
"ds_read_b64_tr_b16 v[64:65], v104 offset:17408\n"
"ds_read_b64_tr_b16 v[66:67], v105 offset:17408\n"
"ds_read_b64_tr_b16 v[68:69], v104 offset:50176\n"
"ds_read_b64_tr_b16 v[70:71], v105 offset:50176\n"
"v_dot2c_f32_bf16 v92, s52, v202\n"
"v_dot2c_f32_bf16 v93, v202, v202\n"
"v_dot2c_f32_bf16 v96, s52, v74\n"
"s_waitcnt lgkmcnt(14)\n"
"v_dot2c_f32_bf16 v97, s52, v82\n"
"v_dot2c_f32_bf16 v100, s52, v203\n"
"v_dot2c_f32_bf16 v101, v203, v203\n"
"v_dot2c_f32_bf16 v96, s52, v75\n"
"v_dot2c_f32_bf16 v97, s52, v83\n"
"s_mul_i32 s101, s32, 98304\n"
"s_add_u32 s60, s60, 1\n"
"s_and_b32 s60, s60, 3\n"
"s_mul_i32 s79, s60, 393216\n"
"s_add_u32 s79, s79, s101\n"
"v_dot2c_f32_bf16 v92, s52, v204\n"
"v_dot2c_f32_bf16 v93, v204, v204\n"
"v_dot2c_f32_bf16 v96, s52, v76\n"
"s_waitcnt lgkmcnt(13)\n"
"v_dot2c_f32_bf16 v97, s52, v84\n"
"v_dot2c_f32_bf16 v100, s52, v205\n"
"v_dot2c_f32_bf16 v101, v205, v205\n"
"v_dot2c_f32_bf16 v96, s52, v77\n"
"v_dot2c_f32_bf16 v97, s52, v85\n"
"v_dot2c_f32_bf16 v92, s52, v206\n"
"v_dot2c_f32_bf16 v93, v206, v206\n"
"v_dot2c_f32_bf16 v96, s52, v78\n"
"s_waitcnt lgkmcnt(12)\n"
"v_dot2c_f32_bf16 v97, s52, v86\n"
"v_dot2c_f32_bf16 v100, s52, v207\n"
"v_dot2c_f32_bf16 v101, v207, v207\n"
"v_dot2c_f32_bf16 v96, s52, v79\n"
"v_dot2c_f32_bf16 v97, s52, v87\n"
"v_dot2c_f32_bf16 v92, s52, v208\n"
"v_dot2c_f32_bf16 v93, v208, v208\n"
"s_waitcnt lgkmcnt(11)\n"
"v_dot2c_f32_bf16 v34, s52, v224\n"
"v_dot2c_f32_bf16 v35, v224, v224\n"
"v_dot2c_f32_bf16 v100, s52, v209\n"
"v_dot2c_f32_bf16 v101, v209, v209\n"
"v_dot2c_f32_bf16 v34, s52, v225\n"
"v_dot2c_f32_bf16 v35, v225, v225\n"
"s_add_u32 m0, s50, 4294934528\n"
"s_add_u32 s78, s79, 48128\n"
"buffer_load_dwordx4 v47, s[16:19], s79 offen offset:0 lds\n"
"v_dot2c_f32_bf16 v92, s52, v210\n"
"v_dot2c_f32_bf16 v93, v210, v210\n"
"v_dot2c_f32_bf16 v34, s52, v226\n"
"v_dot2c_f32_bf16 v35, v226, v226\n"
"v_dot2c_f32_bf16 v100, s52, v211\n"
"v_dot2c_f32_bf16 v101, v211, v211\n"
"v_dot2c_f32_bf16 v34, s52, v227\n"
"v_dot2c_f32_bf16 v35, v227, v227\n"
"v_dot2c_f32_bf16 v92, s52, v212\n"
"v_dot2c_f32_bf16 v93, v212, v212\n"
"s_waitcnt lgkmcnt(10)\n"
"v_dot2c_f32_bf16 v34, s52, v228\n"
"v_dot2c_f32_bf16 v35, v228, v228\n"
"v_dot2c_f32_bf16 v100, s52, v213\n"
"v_dot2c_f32_bf16 v101, v213, v213\n"
"v_dot2c_f32_bf16 v34, s52, v229\n"
"v_dot2c_f32_bf16 v35, v229, v229\n"
"v_dot2c_f32_bf16 v92, s52, v214\n"
"v_dot2c_f32_bf16 v93, v214, v214\n"
"v_dot2c_f32_bf16 v34, s52, v230\n"
"v_dot2c_f32_bf16 v35, v230, v230\n"
"v_dot2c_f32_bf16 v100, s52, v215\n"
"v_dot2c_f32_bf16 v101, v215, v215\n"
"v_dot2c_f32_bf16 v34, s52, v231\n"
"v_dot2c_f32_bf16 v35, v231, v231\n"
"buffer_load_dwordx4 v47, s[16:19], s78 offen offset:1024 lds\n"
"v_dot2c_f32_bf16 v92, s52, v216\n"
"v_dot2c_f32_bf16 v93, v216, v216\n"
"s_waitcnt lgkmcnt(9)\n"
"v_dot2c_f32_bf16 v34, s52, v232\n"
"v_dot2c_f32_bf16 v35, v232, v232\n"
"v_dot2c_f32_bf16 v100, s52, v217\n"
"v_dot2c_f32_bf16 v101, v217, v217\n"
"v_dot2c_f32_bf16 v34, s52, v233\n"
"v_dot2c_f32_bf16 v35, v233, v233\n"
"v_dot2c_f32_bf16 v92, s52, v218\n"
"v_dot2c_f32_bf16 v93, v218, v218\n"
"v_dot2c_f32_bf16 v34, s52, v234\n"
"v_dot2c_f32_bf16 v35, v234, v234\n"
"v_dot2c_f32_bf16 v100, s52, v219\n"
"v_dot2c_f32_bf16 v101, v219, v219\n"
"v_dot2c_f32_bf16 v34, s52, v235\n"
"v_dot2c_f32_bf16 v35, v235, v235\n"
"v_dot2c_f32_bf16 v92, s52, v220\n"
"v_dot2c_f32_bf16 v93, v220, v220\n"
"s_waitcnt lgkmcnt(8)\n"
"v_dot2c_f32_bf16 v34, s52, v236\n"
"v_dot2c_f32_bf16 v35, v236, v236\n"
"v_dot2c_f32_bf16 v100, s52, v221\n"
"v_dot2c_f32_bf16 v101, v221, v221\n"
"v_dot2c_f32_bf16 v34, s52, v237\n"
"v_dot2c_f32_bf16 v35, v237, v237\n"
"v_dot2c_f32_bf16 v92, s52, v222\n"
"v_dot2c_f32_bf16 v93, v222, v222\n"
"v_dot2c_f32_bf16 v34, s52, v238\n"
"v_dot2c_f32_bf16 v35, v238, v238\n"
"v_dot2c_f32_bf16 v100, s52, v223\n"
"v_dot2c_f32_bf16 v101, v223, v223\n"
"v_dot2c_f32_bf16 v34, s52, v239\n"
"v_dot2c_f32_bf16 v35, v239, v239\n"
"ds_read_b128 v[72:75], v110 offset:0\n"
"ds_read_b64_tr_b16 v[76:77], v108 offset:0\n"
"ds_read_b64_tr_b16 v[78:79], v109 offset:0\n"
"ds_read_b128 v[80:83], v111 offset:0\n"
"ds_read_b64_tr_b16 v[84:85], v108 offset:2048\n"
"ds_read_b64_tr_b16 v[86:87], v109 offset:2048\n"
"s_add_u32 s60, s60, 1\n"
"s_and_b32 s60, s60, 3\n"
"s_mul_i32 s79, s60, 393216\n"
"s_add_u32 s79, s79, s101\n"
"s_waitcnt lgkmcnt(12)\n"
"v_mfma_f32_32x32x16_bf16 v[0:15], v[56:59], v[192:195], v[0:15]\n"
"ds_read_b64_tr_b16 v[48:49], v104 offset:18432\n"
"ds_read_b64_tr_b16 v[50:51], v105 offset:18432\n"
"v_lshrrev_b32 v32, 1, v115\n"
"s_waitcnt lgkmcnt(12)\n"
"v_mfma_f32_32x32x16_bf16 v[16:31], v[60:63], v[192:195], v[16:31]\n"
"ds_read_b64_tr_b16 v[52:53], v104 offset:51200\n"
"ds_read_b64_tr_b16 v[54:55], v105 offset:51200\n"
"s_waitcnt lgkmcnt(7)\n"
"v_mfma_f32_16x16x32_bf16 v[88:91], v[76:79], v[72:75], 0\n"
"ds_read_b128 v[72:75], v110 offset:128\n"
"ds_read_b64_tr_b16 v[76:77], v108 offset:4096\n"
"ds_read_b64_tr_b16 v[78:79], v109 offset:4096\n"
"s_add_u32 m0, s50, 4294942720\n"
"s_add_u32 s78, s79, 48128\n"
"buffer_load_dwordx4 v47, s[16:19], s79 offen offset:0 lds\n"
"v_mfma_f32_32x32x16_bf16 v[0:15], v[64:67], v[196:199], v[0:15]\n"
"ds_read_b64_tr_b16 v[56:57], v104 offset:19456\n"
"ds_read_b64_tr_b16 v[58:59], v105 offset:19456\n"
"s_lshl_b32 s34, s32, 9\n"
"s_add_u32 s34, s34, 161792\n"
"v_add_u32 v32, s34, v32\n"
"ds_write_b64 v32, v[34:35]\n"
"v_mfma_f32_32x32x16_bf16 v[16:31], v[68:71], v[196:199], v[16:31]\n"
"ds_read_b64_tr_b16 v[60:61], v104 offset:52224\n"
"ds_read_b64_tr_b16 v[62:63], v105 offset:52224\n"
"s_waitcnt lgkmcnt(12)\n"
"v_mfma_f32_16x16x32_bf16 v[88:91], v[84:87], v[80:83], v[88:91]\n"
"ds_read_b128 v[80:83], v111 offset:128\n"
"ds_read_b64_tr_b16 v[84:85], v108 offset:6144\n"
"ds_read_b64_tr_b16 v[86:87], v109 offset:6144\n"
"buffer_load_dwordx4 v47, s[16:19], s78 offen offset:1024 lds\n"
"s_waitcnt lgkmcnt(13)\n"
"v_mfma_f32_32x32x16_bf16 v[0:15], v[48:51], v[200:203], v[0:15]\n"
"ds_read_b64_tr_b16 v[64:65], v104 offset:20480\n"
"ds_read_b64_tr_b16 v[66:67], v105 offset:20480\n"
"s_waitcnt lgkmcnt(13)\n"
"v_mfma_f32_32x32x16_bf16 v[16:31], v[52:55], v[200:203], v[16:31]\n"
"ds_read_b64_tr_b16 v[68:69], v104 offset:53248\n"
"ds_read_b64_tr_b16 v[70:71], v105 offset:53248\n"
"s_lshl_b32 s35, s32, 2\n"
"v_bfe_u32 v32, v115, 4, 4\n"
"v_xor_b32 v32, s35, v32\n"
"v_bfe_u32 v33, v115, 4, 5\n"
"s_waitcnt lgkmcnt(12)\n"
"v_mfma_f32_16x16x32_bf16 v[88:91], v[76:79], v[72:75], v[88:91]\n"
"v_lshlrev_b32 v33, 8, v33\n"
"v_lshl_add_u32 v32, v32, 4, v33\n"
"s_waitcnt lgkmcnt(10)\n"
"v_mfma_f32_32x32x16_bf16 v[0:15], v[56:59], v[204:207], v[0:15]\n"
"ds_read_b64_tr_b16 v[48:49], v104 offset:21504\n"
"ds_read_b64_tr_b16 v[50:51], v105 offset:21504\n"
"s_add_u32 s60, s60, 1\n"
"s_and_b32 s60, s60, 3\n"
"s_mul_i32 s79, s60, 393216\n"
"s_add_u32 s79, s79, s101\n"
"v_lshrrev_b32 v33, 9, v115\n"
"s_waitcnt lgkmcnt(9)\n"
"v_mfma_f32_32x32x16_bf16 v[16:31], v[60:63], v[204:207], v[16:31]\n"
"ds_read_b64_tr_b16 v[52:53], v104 offset:54272\n"
"ds_read_b64_tr_b16 v[54:55], v105 offset:54272\n"
"v_lshl_add_u32 v32, v33, 3, v32\n"
"s_lshl_b32 s34, s61, 13\n"
"s_add_u32 s34, s34, 114688\n"
"v_add_u32 v32, s34, v32\n"
"s_waitcnt lgkmcnt(8)\n"
"v_mfma_f32_16x16x32_bf16 v[88:91], v[84:87], v[80:83], v[88:91]\n"
"v_xor_b32 v33, 16, v32\n"
"v_xor_b32 v34, 32, v32\n"
"s_waitcnt lgkmcnt(6)\n"
"v_mfma_f32_32x32x16_bf16 v[0:15], v[64:67], v[208:211], v[0:15]\n"
"ds_read_b64_tr_b16 v[56:57], v104 offset:22528\n"
"ds_read_b64_tr_b16 v[58:59], v105 offset:22528\n"
"s_add_u32 m0, s50, 4294950912\n"
"s_add_u32 s78, s79, 48128\n"
"buffer_load_dwordx4 v47, s[16:19], s79 offen offset:0 lds\n"
"v_xor_b32 v35, 48, v32\n"
"s_waitcnt lgkmcnt(6)\n"
"v_mfma_f32_32x32x16_bf16 v[16:31], v[68:71], v[208:211], v[16:31]\n"
"ds_read_b64_tr_b16 v[60:61], v104 offset:55296\n"
"ds_read_b64_tr_b16 v[62:63], v105 offset:55296\n"
"s_waitcnt vmcnt(15)\n"
"ds_read_b64_tr_b16 v[72:73], v44 offset:24576\n"
"s_waitcnt lgkmcnt(7)\n"
"v_mfma_f32_32x32x16_bf16 v[0:15], v[48:51], v[212:215], v[0:15]\n"
"ds_read_b64_tr_b16 v[64:65], v104 offset:23552\n"
"ds_read_b64_tr_b16 v[66:67], v105 offset:23552\n"
"ds_read_b64_tr_b16 v[74:75], v45 offset:24576\n"
"ds_read_b64_tr_b16 v[76:77], v44 offset:25600\n"
"s_waitcnt lgkmcnt(9)\n"
"v_mfma_f32_32x32x16_bf16 v[16:31], v[52:55], v[212:215], v[16:31]\n"
"ds_read_b64_tr_b16 v[68:69], v104 offset:56320\n"
"ds_read_b64_tr_b16 v[70:71], v105 offset:56320\n"
"buffer_load_dwordx4 v47, s[16:19], s78 offen offset:1024 lds\n"
"ds_read_b64_tr_b16 v[78:79], v45 offset:25600\n"
"ds_read_b64_tr_b16 v[80:81], v44 offset:57344\n"
"s_waitcnt lgkmcnt(11)\n"
"v_mfma_f32_32x32x16_bf16 v[0:15], v[56:59], v[216:219], v[0:15]\n"
"ds_read_b64_tr_b16 v[82:83], v45 offset:57344\n"
"ds_read_b64_tr_b16 v[84:85], v44 offset:58368\n"
"s_waitcnt lgkmcnt(11)\n"
"v_mfma_f32_32x32x16_bf16 v[16:31], v[60:63], v[216:219], v[16:31]\n"
"ds_read_b64_tr_b16 v[86:87], v45 offset:58368\n"
"s_waitcnt lgkmcnt(9)\n"
"v_mfma_f32_32x32x16_bf16 v[0:15], v[64:67], v[220:223], v[0:15]\n"
"s_waitcnt lgkmcnt(5)\n"
"v_mfma_f32_32x32x16_bf16 v[16:31], v[68:71], v[220:223], v[16:31]\n"
"v_dot2c_f32_bf16 v96, s52, v72\n"
"s_waitcnt lgkmcnt(3)\n"
"v_dot2c_f32_bf16 v97, s52, v80\n"
"v_dot2c_f32_bf16 v96, s52, v73\n"
"v_dot2c_f32_bf16 v97, s52, v81\n"
"v_dot2c_f32_bf16 v96, s52, v74\n"
"s_waitcnt lgkmcnt(2)\n"
"v_dot2c_f32_bf16 v97, s52, v82\n"
"v_dot2c_f32_bf16 v96, s52, v75\n"
"v_dot2c_f32_bf16 v97, s52, v83\n"
"v_dot2c_f32_bf16 v96, s52, v76\n"
"s_waitcnt lgkmcnt(1)\n"
"v_dot2c_f32_bf16 v97, s52, v84\n"
"v_dot2c_f32_bf16 v96, s52, v77\n"
"v_dot2c_f32_bf16 v97, s52, v85\n"
"v_dot2c_f32_bf16 v96, s52, v78\n"
"s_waitcnt lgkmcnt(0)\n"
"v_dot2c_f32_bf16 v97, s52, v86\n"
"v_dot2c_f32_bf16 v96, s52, v79\n"
"v_dot2c_f32_bf16 v97, s52, v87\n"
"s_nop 1\n"
"v_mov_b32 v72, v96\n"
"v_mov_b32 v73, v97\n"
"v_bfe_u32 v75, v115, 4, 5\n"
"v_permlane32_swap_b32 v96, v72\n"
"v_add_f32 v96, v96, v72\n"
"v_permlane32_swap_b32 v97, v73\n"
"v_add_f32 v97, v97, v73\n"
"s_lshl_b32 s34, s32, 2\n"
"s_add_u32 s34, s34, 159744\n"
"v_lshl_add_u32 v76, v75, 4, s34\n"
"ds_write_b32 v76, v96 offset:0\n"
"ds_write_b32 v76, v97 offset:512\n"
"s_waitcnt vmcnt(8)\n"
"ds_read_b128 v[224:227], v36 offset:49152\n"
"ds_read_b128 v[228:231], v37 offset:49152\n"
"ds_read_b128 v[232:235], v38 offset:49152\n"
"ds_read_b128 v[236:239], v39 offset:49152\n"
"ds_read_b128 v[240:243], v40 offset:49152\n"
"ds_read_b128 v[244:247], v41 offset:49152\n"
"ds_read_b128 v[248:251], v42 offset:49152\n"
"ds_read_b128 v[252:255], v43 offset:49152\n"
"s_waitcnt lgkmcnt(0)\n"
"s_barrier\n"
"v_bfe_u32 v75, v115, 4, 5\n"
"s_and_b32 s34, s32, 1\n"
"s_lshl_b32 s34, s34, 9\n"
"s_add_u32 s34, s34, 159744\n"
"v_lshl_add_u32 v76, v75, 4, s34\n"
"ds_read_b128 v[78:81], v76\n"
"s_mov_b32 s34, s32\n"
"s_lshl_b32 s34, s34, 9\n"
"s_add_u32 s34, s34, 147456\n"
"v_lshl_add_u32 v76, v75, 4, s34\n"
"ds_read_b64 a[180:181], v32\n"
"ds_read_b64 a[182:183], v33\n"
"ds_read_b64 a[184:185], v34\n"
"ds_read_b64 a[186:187], v35\n"
"v_lshrrev_b32 v36, 1, v115\n"
"v_add_u32 v36, 161792, v36\n"
"ds_read_b64 v[38:39], v36 offset:0\n"
"ds_read_b64 v[40:41], v36 offset:512\n"
"ds_read_b64 v[42:43], v36 offset:1024\n"
"ds_read_b64 v[36:37], v36 offset:1536\n"
"s_waitcnt lgkmcnt(8)\n"
"v_add_f32 v78, v78, v79\n"
"v_add_f32 v80, v80, v81\n"
"v_add_f32 v78, v78, v80\n"
"v_cvt_pk_bf16_f32 v79, v78, v78\n"
"v_lshlrev_b32 v79, 16, v79\n"
"v_sub_f32 v80, v78, v79\n"
"v_cvt_pk_bf16_f32 v78, v79, v80\n"
"v_and_b32 v81, 0xffff0000, v78\n"
"v_sub_f32 v80, v80, v81\n"
"v_mov_b32 v81, 0\n"
"v_cvt_pk_bf16_f32 v79, v80, v81\n"
"v_mov_b32 v80, 0\n"
"ds_write_b128 v76, v[78:81]\n"
"v_dot2c_f32_bf16 v92, s52, v224\n"
"v_dot2c_f32_bf16 v93, v224, v224\n"
"v_dot2c_f32_bf16 v100, s52, v225\n"
"v_dot2c_f32_bf16 v101, v225, v225\n"
"v_dot2c_f32_bf16 v92, s52, v226\n"
"v_dot2c_f32_bf16 v93, v226, v226\n"
"v_dot2c_f32_bf16 v100, s52, v227\n"
"v_dot2c_f32_bf16 v101, v227, v227\n"
"s_add_u32 s60, s60, 1\n"
"s_and_b32 s60, s60, 3\n"
"s_mul_i32 s79, s60, 393216\n"
"s_add_u32 s79, s79, s101\n"
"s_add_u32 m0, s50, 4294959104\n"
"s_add_u32 s78, s79, 48128\n"
"buffer_load_dwordx4 v47, s[16:19], s79 offen offset:0 lds\n"
"v_dot2c_f32_bf16 v92, s52, v228\n"
"v_dot2c_f32_bf16 v93, v228, v228\n"
"v_dot2c_f32_bf16 v100, s52, v229\n"
"v_dot2c_f32_bf16 v101, v229, v229\n"
"v_dot2c_f32_bf16 v92, s52, v230\n"
"v_dot2c_f32_bf16 v93, v230, v230\n"
"v_dot2c_f32_bf16 v100, s52, v231\n"
"v_dot2c_f32_bf16 v101, v231, v231\n"
"ds_read_b64_tr_b16 v[48:49], v104 offset:24576\n"
"ds_read_b64_tr_b16 v[50:51], v105 offset:24576\n"
"ds_read_b64_tr_b16 v[52:53], v104 offset:57344\n"
"ds_read_b64_tr_b16 v[54:55], v105 offset:57344\n"
"buffer_load_dwordx4 v47, s[16:19], s78 offen offset:1024 lds\n"
"v_dot2c_f32_bf16 v92, s52, v232\n"
"v_dot2c_f32_bf16 v93, v232, v232\n"
"v_dot2c_f32_bf16 v100, s52, v233\n"
"v_dot2c_f32_bf16 v101, v233, v233\n"
"v_dot2c_f32_bf16 v92, s52, v234\n"
"v_dot2c_f32_bf16 v93, v234, v234\n"
"v_dot2c_f32_bf16 v100, s52, v235\n"
"v_dot2c_f32_bf16 v101, v235, v235\n"
"ds_read_b64_tr_b16 v[56:57], v104 offset:25600\n"
"ds_read_b64_tr_b16 v[58:59], v105 offset:25600\n"
"ds_read_b64_tr_b16 v[60:61], v104 offset:58368\n"
"ds_read_b64_tr_b16 v[62:63], v105 offset:58368\n"
"v_add_u32 v44, s33, v112\n"
"s_add_u32 s34, s50, 8192\n"
"v_add_u32 v45, s34, v112\n"
"v_dot2c_f32_bf16 v92, s52, v236\n"
"v_dot2c_f32_bf16 v93, v236, v236\n"
"v_dot2c_f32_bf16 v100, s52, v237\n"
"v_dot2c_f32_bf16 v101, v237, v237\n"
"v_dot2c_f32_bf16 v92, s52, v238\n"
"v_dot2c_f32_bf16 v93, v238, v238\n"
"v_dot2c_f32_bf16 v100, s52, v239\n"
"v_dot2c_f32_bf16 v101, v239, v239\n"
"ds_read_b64_tr_b16 a[16:17], v44 offset:0\n"
"ds_read_b64_tr_b16 a[18:19], v44 offset:512\n"
"v_dot2c_f32_bf16 v92, s52, v240\n"
"v_dot2c_f32_bf16 v93, v240, v240\n"
"v_dot2c_f32_bf16 v100, s52, v241\n"
"v_dot2c_f32_bf16 v101, v241, v241\n"
"v_dot2c_f32_bf16 v92, s52, v242\n"
"v_dot2c_f32_bf16 v93, v242, v242\n"
"v_dot2c_f32_bf16 v100, s52, v243\n"
"v_dot2c_f32_bf16 v101, v243, v243\n"
"s_waitcnt vmcnt(8)\n"
"ds_read_b64_tr_b16 a[8:9], v45 offset:0\n"
"ds_read_b64_tr_b16 a[10:11], v45 offset:512\n"
"v_dot2c_f32_bf16 v92, s52, v244\n"
"v_dot2c_f32_bf16 v93, v244, v244\n"
"v_dot2c_f32_bf16 v100, s52, v245\n"
"v_dot2c_f32_bf16 v101, v245, v245\n"
"v_dot2c_f32_bf16 v92, s52, v246\n"
"v_dot2c_f32_bf16 v93, v246, v246\n"
"v_dot2c_f32_bf16 v100, s52, v247\n"
"v_dot2c_f32_bf16 v101, v247, v247\n"
"ds_read_b64_tr_b16 a[12:13], v45 offset:1024\n"
"ds_read_b64_tr_b16 a[14:15], v45 offset:1536\n"
"s_waitcnt lgkmcnt(4)\n"
"buffer_store_dwordx4 a[16:19], v115, s[8:11], s100 offen sc1\n"
"v_dot2c_f32_bf16 v92, s52, v248\n"
"v_dot2c_f32_bf16 v93, v248, v248\n"
"v_dot2c_f32_bf16 v100, s52, v249\n"
"v_dot2c_f32_bf16 v101, v249, v249\n"
"v_dot2c_f32_bf16 v92, s52, v250\n"
"v_dot2c_f32_bf16 v93, v250, v250\n"
"v_dot2c_f32_bf16 v100, s52, v251\n"
"v_dot2c_f32_bf16 v101, v251, v251\n"
"s_waitcnt lgkmcnt(2)\n"
"buffer_store_dwordx4 a[8:11], v115, s[8:11], s54 offen offset:0 sc1\n"
"v_dot2c_f32_bf16 v92, s52, v252\n"
"v_dot2c_f32_bf16 v93, v252, v252\n"
"v_dot2c_f32_bf16 v100, s52, v253\n"
"v_dot2c_f32_bf16 v101, v253, v253\n"
"v_dot2c_f32_bf16 v92, s52, v254\n"
"v_dot2c_f32_bf16 v93, v254, v254\n"
"v_dot2c_f32_bf16 v100, s52, v255\n"
"v_dot2c_f32_bf16 v101, v255, v255\n"
"ds_read_b128 v[72:75], v110 offset:512\n"
"ds_read_b64_tr_b16 v[76:77], v108 offset:16384\n"
"ds_read_b64_tr_b16 v[78:79], v109 offset:16384\n"
"ds_read_b128 v[80:83], v111 offset:512\n"
"ds_read_b64_tr_b16 v[84:85], v108 offset:18432\n"
"ds_read_b64_tr_b16 v[86:87], v109 offset:18432\n"
"s_waitcnt lgkmcnt(6)\n"
"buffer_store_dwordx4 a[12:15], v115, s[8:11], s54 offen offset:1024 sc1\n"
"v_mfma_f32_32x32x16_bf16 v[0:15], v[48:51], v[224:227], v[0:15]\n"
"ds_read_b64_tr_b16 v[64:65], v104 offset:26624\n"
"ds_read_b64_tr_b16 v[66:67], v105 offset:26624\n"
"v_mov_b32 v112, v115\n"
"v_add_f32 v36, v36, v38\n"
"v_add_f32 v37, v37, v39\n"
"v_add_f32 v40, v40, v42\n"
"v_add_f32 v41, v41, v43\n"
"v_mfma_f32_32x32x16_bf16 v[16:31], v[52:55], v[224:227], v[16:31]\n"
"ds_read_b64_tr_b16 v[68:69], v104 offset:59392\n"
"ds_read_b64_tr_b16 v[70:71], v105 offset:59392\n"
"v_add_u32 v113, 0x10000, v115\n"
"v_add_f32 v36, v36, v40\n"
"v_add_f32 v37, v37, v41\n"
"v_add_f32 v92, v92, v100\n"
"v_add_f32 v93, v93, v101\n"
"s_waitcnt lgkmcnt(7)\n"
"v_mfma_f32_16x16x32_bf16 v[88:91], v[76:79], v[72:75], v[88:91]\n"
"ds_read_b128 v[72:75], v110 offset:640\n"
"ds_read_b64_tr_b16 v[76:77], v108 offset:20480\n"
"ds_read_b64_tr_b16 v[78:79], v109 offset:20480\n"
"s_add_u32 s34, s44, 0\n"
"buffer_store_dwordx4 a[0:3], v115, s[8:11], s34 offen offset:0 sc1\n"
"s_add_u32 s34, s44, 0\n"
"buffer_store_dwordx4 a[0:3], v115, s[8:11], s34 offen offset:1024 sc1\n"
"v_permlane32_swap_b32 v92, v93\n"
"v_permlane32_swap_b32 v36, v37\n"
"v_mfma_f32_32x32x16_bf16 v[0:15], v[56:59], v[228:231], v[0:15]\n"
"ds_read_b64_tr_b16 v[48:49], v104 offset:27648\n"
"ds_read_b64_tr_b16 v[50:51], v105 offset:27648\n"
"s_cmp_lg_u32 s61, 3\n"
"s_cbranch_scc1 L_px3\n"
"s_mov_b32 s34, s46\n"
"buffer_store_dwordx4 a[0:3], v115, s[8:11], s34 offen offset:0 sc1\n"
"buffer_store_dwordx4 a[0:3], v115, s[8:11], s34 offen offset:1024 sc1\n"
"L_px3:\n"
"v_mov_b32 v38, 0\n"
"v_add_f32 v36, v36, v37\n"
"v_mfma_f32_32x32x16_bf16 v[16:31], v[60:63], v[228:231], v[16:31]\n"
"ds_read_b64_tr_b16 v[52:53], v104 offset:60416\n"
"ds_read_b64_tr_b16 v[54:55], v105 offset:60416\n"
"v_bfe_u32 v44, v115, 4, 5\n"
"v_lshrrev_b32 v45, 9, v115\n"
"v_lshlrev_b32 v44, 5, v44\n"
"v_lshl_add_u32 v44, v45, 2, v44\n"
"s_cmp_eq_u32 s32, 0\n"
"s_cselect_b32 s35, 0, 1073741824\n"
"v_or_b32 v44, s35, v44\n"
"s_add_u32 s34, s47, 0\n"
"buffer_store_dword a0, v44, s[8:11], s34 offen sc1\n"
"v_add_f32 v92, v92, v93\n"
"v_mov_b32 v100, 0\n"
"s_waitcnt lgkmcnt(11)\n"
"v_mfma_f32_16x16x32_bf16 v[88:91], v[84:87], v[80:83], v[88:91]\n"
"ds_read_b128 v[80:83], v111 offset:640\n"
"ds_read_b64_tr_b16 v[84:85], v108 offset:22528\n"
"ds_read_b64_tr_b16 v[86:87], v109 offset:22528\n"
"s_cmp_lg_u32 s32, 0\n"
"s_cbranch_scc1 L_hp2\n"
"v_accvgpr_read_b32 v44, a131\n"
"s_mov_b64 exec, s[76:77]\n"
"buffer_load_dwordx2 a[4:5], v44, s[8:11], 0 offen sc1\n"
"s_mov_b64 exec, -1\n"
"L_hp2:\n"
"v_permlane16_swap_b32 v36, v38\n"
"v_add_f32 v36, v36, v38\n"
"s_waitcnt lgkmcnt(12)\n"
"v_mfma_f32_32x32x16_bf16 v[0:15], v[64:67], v[232:235], v[0:15]\n"
"ds_read_b64_tr_b16 v[56:57], v104 offset:28672\n"
"ds_read_b64_tr_b16 v[58:59], v105 offset:28672\n"
"s_lshl_b32 s34, s58, 17\n"
"s_add_u32 s34, s34, s48\n"
"s_add_u32 s34, s34, 18874368\n"
"v_add_u32 v126, s34, v115\n"
"v_permlane32_swap_b32 v92, v36\n"
"v_mul_f32 v92, 0xbb000000, v92\n"
"s_waitcnt lgkmcnt(12)\n"
"v_mfma_f32_32x32x16_bf16 v[16:31], v[68:71], v[232:235], v[16:31]\n"
"ds_read_b64_tr_b16 v[60:61], v104 offset:61440\n"
"ds_read_b64_tr_b16 v[62:63], v105 offset:61440\n"
"v_mul_f32 v36, 0x3b000000, v36\n"
"v_fma_f32 v36, -v92, v92, v36\n"
"s_waitcnt lgkmcnt(11)\n"
"v_mfma_f32_16x16x32_bf16 v[88:91], v[76:79], v[72:75], v[88:91]\n"
"v_cvt_pk_bf16_f32 v96, v92, v92\n"
"v_max_f32 v36, 0, v36\n"
"s_waitcnt lgkmcnt(9)\n"
"v_mfma_f32_32x32x16_bf16 v[0:15], v[48:51], v[236:239], v[0:15]\n"
"ds_read_b64_tr_b16 v[64:65], v104 offset:29696\n"
"ds_read_b64_tr_b16 v[66:67], v105 offset:29696\n"
"v_cvt_pk_bf16_f32 v36, v36, v36\n"
"v_permlane32_swap_b32 v96, v100\n"
"s_waitcnt lgkmcnt(9)\n"
"v_mfma_f32_32x32x16_bf16 v[16:31], v[52:55], v[236:239], v[16:31]\n"
"ds_read_b64_tr_b16 v[68:69], v104 offset:62464\n"
"ds_read_b64_tr_b16 v[70:71], v105 offset:62464\n"
"v_lshlrev_b32 v36, 16, v36\n"
"v_add_f32 v36, 0x3727c5ac, v36\n"
"s_waitcnt lgkmcnt(8)\n"
"v_mfma_f32_16x16x32_bf16 v[88:91], v[84:87], v[80:83], v[88:91]\n"
"v_and_b32 v97, 0xffff, v96\n"
"v_cvt_pk_bf16_f32 v36, v36, v36\n"
"s_waitcnt lgkmcnt(6)\n"
"v_mfma_f32_32x32x16_bf16 v[0:15], v[56:59], v[240:243], v[0:15]\n"
"ds_read_b64_tr_b16 v[48:49], v104 offset:30720\n"
"ds_read_b64_tr_b16 v[50:51], v105 offset:30720\n"
"v_and_b32 v101, 0xffff, v100\n"
"v_lshlrev_b32 v36, 16, v36\n"
"s_waitcnt lgkmcnt(6)\n"
"v_mfma_f32_32x32x16_bf16 v[16:31], v[60:63], v[240:243], v[16:31]\n"
"ds_read_b64_tr_b16 v[52:53], v104 offset:63488\n"
"ds_read_b64_tr_b16 v[54:55], v105 offset:63488\n"
"v_sqrt_f32 v36, v36\n"
"s_nop 0\n"
"v_cvt_pk_bf16_f32 v36, v36, v36\n"
"s_cmp_lt_u32 s32, 2\n"
"s_cbranch_scc1 L_hw4\n"
"s_and_b32 s34, s32, 1\n"
"s_lshl_b32 s34, s34, 10\n"
"v_add_u32 v72, s34, v94\n"
"ds_write_b128 v72, v[88:91] offset:2048\n"
"L_hw4:\n"
"s_waitcnt lgkmcnt(0)\n"
"s_barrier\n"
"v_mfma_f32_32x32x16_bf16 v[0:15], v[64:67], v[244:247], v[0:15]\n"
"ds_read_b64_tr_b16 v[56:57], v104 offset:31744\n"
"ds_read_b64_tr_b16 v[58:59], v105 offset:31744\n"
"v_lshlrev_b32 v36, 16, v36\n"
"v_rcp_f32 v93, v36\n"
"v_mov_b32 v32, 0xff800000\n"
"v_mov_b32 v33, 0xff800000\n"
"v_mov_b32 v34, 147088\n"
"ds_write_b64 v34, v[32:33]\n"
"ds_write_b64 v34, v[32:33] offset:8\n"
"s_and_b32 s34, s32, 1\n"
"s_lshl_b32 s34, s34, 10\n"
"v_add_u32 v72, s34, v94\n"
"ds_read_b128 v[72:75], v72 offset:2048\n"
"v_mfma_f32_32x32x16_bf16 v[16:31], v[68:71], v[244:247], v[16:31]\n"
"ds_read_b64_tr_b16 v[60:61], v104 offset:64512\n"
"ds_read_b64_tr_b16 v[62:63], v105 offset:64512\n"
"ds_write_b32 v127, v93\n"
"v_mul_f32 v93, 0x3eb8aa3b, v93\n"
"ds_read_b128 v[40:43], v94 offset:512\n"
"v_and_b32 v39, 0xfffffcff, v94\n"
"v_mfma_f32_32x32x16_bf16 v[0:15], v[48:51], v[248:251], v[0:15]\n"
"v_mov_b32 v38, v93\n"
"s_nop 1\n"
"v_permlane32_swap_b32 v93, v38\n"
"ds_read_b128 v[44:47], v94\n"
"v_add_u32 v39, s53, v39\n"
"v_mfma_f32_32x32x16_bf16 v[16:31], v[52:55], v[248:251], v[16:31]\n"
"ds_read_b128 v[32:35], v39\n"
"s_waitcnt lgkmcnt(9)\n"
"v_mfma_f32_32x32x16_bf16 v[0:15], v[56:59], v[252:255], v[0:15]\n"
"s_waitcnt lgkmcnt(6)\n"
"v_add_f32 v88, v88, v72\n"
"v_add_f32 v89, v89, v73\n"
"s_waitcnt lgkmcnt(4)\n"
"v_mfma_f32_32x32x16_bf16 v[16:31], v[60:63], v[252:255], v[16:31]\n"
"v_add_f32 v90, v90, v74\n"
"v_add_f32 v91, v91, v75\n"
"s_mov_b32 s55, 3072\n"
"s_mov_b32 s56, 6144\n"
"s_mov_b32 s63, 9216\n"
"s_mov_b32 s12, 48128\n"
"s_mov_b32 s13, 96256\n"
"s_mov_b32 s14, 144384\n"
"L_t0:\n"
"ds_read_b128 v[72:75], v116 offset:53248\n"
"ds_read_b128 v[76:79], v116 offset:54272\n"
"ds_read_b128 v[80:83], v116 offset:55296\n"
"ds_read_b128 v[84:87], v116 offset:56320\n"
"s_waitcnt lgkmcnt(4)\n"
"v_mfma_f32_16x16x32_bf16 v[88:91], v[32:35], v[100:103], v[88:91]\n"
"v_mfma_f32_32x32x16_bf16 v[16:31], v[40:43], v[96:99], v[16:31]\n"
"v_mfma_f32_32x32x16_bf16 v[0:15], v[44:47], v[96:99], v[0:15]\n"
"ds_read_b128 a[176:179], v95\n"
"v_add_u32 v104, s48, v106\n"
"v_add_u32 v105, s48, v107\n"
"v_mov_b32 v92, 0\n"
"s_lshr_b32 s34, s32, 1\n"
"s_lshl_b32 s35, s34, 8\n"
"s_lshl_b32 s34, s34, 13\n"
"s_add_u32 s34, s34, 32768\n"
"v_add_u32 v108, s34, v108\n"
"v_add_u32 v109, s34, v109\n"
"v_add_u32 v110, s35, v110\n"
"v_add_u32 v111, s35, v111\n"
"v_cvt_pk_bf16_f32 v88, v88, v89\n"
"v_cvt_pk_bf16_f32 v89, v90, v91\n"
"s_cmp_lt_u32 s32, 2\n"
"s_cbranch_scc0 L_hd6\n"
"ds_write_b64 v121, v[88:89]\n"
"L_hd6:\n"
"v_cvt_pk_bf16_f32 v16, v16, v17\n"
"v_cvt_pk_bf16_f32 v17, v18, v19\n"
"v_cvt_pk_bf16_f32 v18, v20, v21\n"
"v_cvt_pk_bf16_f32 v19, v22, v23\n"
"v_cvt_pk_bf16_f32 v20, v24, v25\n"
"v_cvt_pk_bf16_f32 v21, v26, v27\n"
"v_cvt_pk_bf16_f32 v22, v28, v29\n"
"v_cvt_pk_bf16_f32 v23, v30, v31\n"
"ds_write_b128 v117, v[16:19] offset:35840\n"
"ds_write_b128 v117, v[20:23] offset:36864\n"
"v_cvt_pk_bf16_f32 v24, v0, v1\n"
"v_cvt_pk_bf16_f32 v25, v2, v3\n"
"v_cvt_pk_bf16_f32 v26, v4, v5\n"
"v_cvt_pk_bf16_f32 v27, v6, v7\n"
"v_cvt_pk_bf16_f32 v28, v8, v9\n"
"v_cvt_pk_bf16_f32 v29, v10, v11\n"
"v_cvt_pk_bf16_f32 v30, v12, v13\n"
"v_cvt_pk_bf16_f32 v31, v14, v15\n"
"s_waitcnt lgkmcnt(3)\n"
"v_mfma_f32_32x32x16_bf16 v[0:15], v[16:19], v[24:27], v[72:87]\n"
"s_waitcnt vmcnt(6)\n"
"s_waitcnt lgkmcnt(0)\n"
"s_barrier\n"
"ds_read_b64_tr_b16 v[48:49], v106 offset:0\n"
"ds_read_b64_tr_b16 v[50:51], v107 offset:0\n"
"ds_read_b128 v[64:67], v117 offset:32768\n"
"ds_read_b128 v[68:71], v117 offset:33792\n"
"v_mfma_f32_32x32x16_bf16 v[72:87], v[20:23], v[28:31], v[72:87]\n"
"ds_read_b64_tr_b16 v[52:53], v106 offset:1024\n"
"ds_read_b64_tr_b16 v[54:55], v107 offset:1024\n"
"ds_read_b32 v60, v122\n"
"ds_read_b32 v61, v123\n"
"ds_read_b32 v62, v124\n"
"ds_read_b32 v63, v125\n"
"ds_read_b64_tr_b16 v[56:57], v106 offset:2048\n"
"ds_read_b64_tr_b16 v[58:59], v107 offset:2048\n"
"ds_read_b128 a[188:191], v110 offset:0\n"
"ds_read_b64_tr_b16 a[192:193], v108 offset:0\n"
"ds_read_b64_tr_b16 a[194:195], v109 offset:0\n"
"ds_read_b128 a[196:199], v111 offset:0\n"
"ds_read_b64_tr_b16 a[200:201], v108 offset:2048\n"
"ds_read_b64_tr_b16 a[202:203], v109 offset:2048\n"
"s_waitcnt lgkmcnt(15)\n"
"v_mfma_f32_32x32x16_bf16 v[32:47], v[128:131], v[48:51], 0\n"
"ds_read_b64_tr_b16 v[48:49], v106 offset:3072\n"
"ds_read_b64_tr_b16 v[50:51], v107 offset:3072\n"
"v_mfma_f32_32x32x16_bf16 a[132:147], v[64:67], v[24:27], a[164:179]\n"
"s_waitcnt lgkmcnt(14)\n"
"v_mfma_f32_32x32x16_bf16 v[32:47], v[132:135], v[52:55], v[32:47]\n"
"ds_read_b64_tr_b16 v[52:53], v106 offset:4096\n"
"ds_read_b64_tr_b16 v[54:55], v107 offset:4096\n"
"s_waitcnt lgkmcnt(15)\n"
"v_mul_f32 v16, v60, v93\n"
"s_waitcnt lgkmcnt(14)\n"
"v_mul_f32 v17, v61, v93\n"
"s_waitcnt lgkmcnt(13)\n"
"v_mul_f32 v18, v62, v93\n"
"s_add_u32 m0, s48, 0\n"
"s_nop 0\n"
"buffer_load_dwordx4 v119, s[24:27], 0 offen offset:0 lds\n"
"v_mfma_f32_32x32x16_bf16 a[148:163], v[68:71], v[28:31], a[164:179]\n"
"s_waitcnt lgkmcnt(12)\n"
"v_mul_f32 v19, v63, v93\n"
"v_max3_f32 v20, v0, v4, v8\n"
"v_max3_f32 v21, v1, v5, v9\n"
"buffer_load_dwordx4 v119, s[24:27], s55 offen offset:1024 lds\n"
"s_waitcnt lgkmcnt(10)\n"
"v_mfma_f32_32x32x16_bf16 v[32:47], v[136:139], v[56:59], v[32:47]\n"
"ds_read_b64_tr_b16 v[56:57], v106 offset:5120\n"
"ds_read_b64_tr_b16 v[58:59], v107 offset:5120\n"
"v_max3_f32 v22, v2, v6, v10\n"
"v_max3_f32 v23, v3, v7, v11\n"
"s_waitcnt lgkmcnt(9)\n"
"v_mfma_f32_16x16x32_bf16 v[88:91], a[192:195], a[188:191], 0\n"
"ds_read_b128 a[188:191], v110 offset:128\n"
"ds_read_b64_tr_b16 a[192:193], v108 offset:4096\n"
"ds_read_b64_tr_b16 a[194:195], v109 offset:4096\n"
"buffer_load_dwordx4 v119, s[24:27], s56 offen offset:2048 lds\n"
"s_waitcnt lgkmcnt(7)\n"
"v_mfma_f32_32x32x16_bf16 v[32:47], v[140:143], v[48:51], v[32:47]\n"
"ds_read_b64_tr_b16 v[48:49], v106 offset:6144\n"
"ds_read_b64_tr_b16 v[50:51], v107 offset:6144\n"
"v_accvgpr_read_b32 v24, a144\n"
"v_accvgpr_read_b32 v25, a145\n"
"v_accvgpr_read_b32 v26, a146\n"
"v_mfma_f32_16x16x32_bf16 v[88:91], a[200:203], a[196:199], v[88:91]\n"
"ds_read_b128 a[196:199], v111 offset:128\n"
"ds_read_b64_tr_b16 a[200:201], v108 offset:6144\n"
"ds_read_b64_tr_b16 a[202:203], v109 offset:6144\n"
"v_accvgpr_read_b32 v27, a147\n"
"v_max3_f32 v20, v20, v12, v24\n"
"v_max3_f32 v21, v21, v13, v25\n"
"buffer_load_dwordx4 v119, s[24:27], s63 offen offset:3072 lds\n"
"s_waitcnt lgkmcnt(10)\n"
"v_mfma_f32_32x32x16_bf16 v[32:47], v[144:147], v[52:55], v[32:47]\n"
"ds_read_b64_tr_b16 v[52:53], v106 offset:7168\n"
"ds_read_b64_tr_b16 v[54:55], v107 offset:7168\n"
"v_max3_f32 v22, v22, v14, v26\n"
"v_max3_f32 v23, v23, v15, v27\n"
"s_add_u32 m0, s48, 4096\n"
"v_add_u32 v119, 16384, v119\n"
"buffer_load_dwordx4 v119, s[24:27], 0 offen offset:0 lds\n"
"s_waitcnt lgkmcnt(7)\n"
"v_mfma_f32_16x16x32_bf16 v[88:91], a[192:195], a[188:191], v[88:91]\n"
"ds_read_b128 a[188:191], v110 offset:256\n"
"ds_read_b64_tr_b16 a[192:193], v108 offset:8192\n"
"ds_read_b64_tr_b16 a[194:195], v109 offset:8192\n"
"v_mul_f32 v20, v20, v16\n"
"v_mul_f32 v21, v21, v17\n"
"v_mul_f32 v22, v22, v18\n"
"v_mfma_f32_32x32x16_bf16 v[32:47], v[148:151], v[56:59], v[32:47]\n"
"ds_read_b64_tr_b16 v[56:57], v106 offset:8192\n"
"ds_read_b64_tr_b16 v[58:59], v107 offset:8192\n"
"v_mul_f32 v23, v23, v19\n"
"v_max3_f32 v68, v20, v21, v22\n"
"buffer_load_dwordx4 v119, s[24:27], s55 offen offset:1024 lds\n"
"s_waitcnt lgkmcnt(7)\n"
"v_mfma_f32_16x16x32_bf16 v[88:91], a[200:203], a[196:199], v[88:91]\n"
"ds_read_b128 a[196:199], v111 offset:256\n"
"ds_read_b64_tr_b16 a[200:201], v108 offset:10240\n"
"ds_read_b64_tr_b16 a[202:203], v109 offset:10240\n"
"v_max_f32 v68, v68, v23\n"
"v_accvgpr_read_b32 v28, a160\n"
"v_accvgpr_read_b32 v29, a161\n"
"v_mfma_f32_32x32x16_bf16 v[32:47], v[152:155], v[48:51], v[32:47]\n"
"ds_read_b64_tr_b16 v[48:49], v106 offset:9216\n"
"ds_read_b64_tr_b16 v[50:51], v107 offset:9216\n"
"v_accvgpr_read_b32 v30, a162\n"
"v_accvgpr_read_b32 v31, a163\n"
"buffer_load_dwordx4 v119, s[24:27], s56 offen offset:2048 lds\n"
"s_waitcnt lgkmcnt(7)\n"
"v_mfma_f32_16x16x32_bf16 v[88:91], a[192:195], a[188:191], v[88:91]\n"
"ds_read_b128 a[188:191], v110 offset:384\n"
"ds_read_b64_tr_b16 a[192:193], v108 offset:12288\n"
"ds_read_b64_tr_b16 a[194:195], v109 offset:12288\n"
"v_max3_f32 v64, v72, v76, v80\n"
"v_max3_f32 v65, v73, v77, v81\n"
"v_max3_f32 v66, v74, v78, v82\n"
"buffer_load_dwordx4 v119, s[24:27], s63 offen offset:3072 lds\n"
"v_mfma_f32_32x32x16_bf16 v[32:47], v[156:159], v[52:55], v[32:47]\n"
"ds_read_b64_tr_b16 v[52:53], v106 offset:10240\n"
"ds_read_b64_tr_b16 v[54:55], v107 offset:10240\n"
"v_max3_f32 v67, v75, v79, v83\n"
"v_max3_f32 v64, v64, v84, v28\n"
"s_waitcnt lgkmcnt(7)\n"
"v_mfma_f32_16x16x32_bf16 v[88:91], a[200:203], a[196:199], v[88:91]\n"
"ds_read_b128 a[196:199], v111 offset:384\n"
"ds_read_b64_tr_b16 a[200:201], v108 offset:14336\n"
"ds_read_b64_tr_b16 a[202:203], v109 offset:14336\n"
"v_max3_f32 v65, v65, v85, v29\n"
"v_max3_f32 v66, v66, v86, v30\n"
"v_max3_f32 v67, v67, v87, v31\n"
"s_add_u32 m0, s48, 32768\n"
"v_add_u32 v119, 114688, v119\n"
"buffer_load_dwordx4 v119, s[24:27], 0 offen offset:0 lds\n"
"v_mfma_f32_32x32x16_bf16 v[32:47], v[160:163], v[56:59], v[32:47]\n"
"ds_read_b64_tr_b16 v[56:57], v106 offset:11264\n"
"ds_read_b64_tr_b16 v[58:59], v107 offset:11264\n"
"ds_read_b64_tr_b16 v[128:129], v104 offset:0\n"
"ds_read_b64_tr_b16 v[130:131], v105 offset:0\n"
"ds_read_b64_tr_b16 v[132:133], v104 offset:1024\n"
"ds_read_b64_tr_b16 v[134:135], v105 offset:1024\n"
"v_mul_f32 v64, v64, v16\n"
"v_mul_f32 v65, v65, v17\n"
"s_waitcnt lgkmcnt(11)\n"
"v_mfma_f32_16x16x32_bf16 v[88:91], a[192:195], a[188:191], v[88:91]\n"
"ds_read_b64_tr_b16 v[136:137], v104 offset:2048\n"
"ds_read_b64_tr_b16 v[138:139], v105 offset:2048\n"
"ds_read_b64_tr_b16 v[140:141], v104 offset:3072\n"
"ds_read_b64_tr_b16 v[142:143], v105 offset:3072\n"
"v_mul_f32 v66, v66, v18\n"
"v_mul_f32 v67, v67, v19\n"
"v_max3_f32 v69, v64, v65, v66\n"
"buffer_load_dwordx4 v119, s[24:27], s55 offen offset:1024 lds\n"
"v_mfma_f32_32x32x16_bf16 v[32:47], v[164:167], v[48:51], v[32:47]\n"
"ds_read_b64_tr_b16 v[48:49], v106 offset:12288\n"
"ds_read_b64_tr_b16 v[50:51], v107 offset:12288\n"
"ds_read_b64_tr_b16 v[144:145], v104 offset:4096\n"
"ds_read_b64_tr_b16 v[146:147], v105 offset:4096\n"
"ds_read_b64_tr_b16 v[148:149], v104 offset:5120\n"
"ds_read_b64_tr_b16 v[150:151], v105 offset:5120\n"
"v_max_f32 v69, v69, v67\n"
"s_nop 1\n"
"v_permlane32_swap_b32 v68, v69\n"
"buffer_load_dwordx4 v119, s[24:27], s56 offen offset:2048 lds\n"
"s_waitcnt lgkmcnt(15)\n"
"v_mfma_f32_16x16x32_bf16 v[88:91], a[200:203], a[196:199], v[88:91]\n"
"ds_read_b64_tr_b16 v[152:153], v104 offset:6144\n"
"ds_read_b64_tr_b16 v[154:155], v105 offset:6144\n"
"ds_read_b64_tr_b16 v[156:157], v104 offset:7168\n"
"ds_read_b64_tr_b16 v[158:159], v105 offset:7168\n"
"v_max_f32 v68, v68, v69\n"
"v_mov_b32 v69, v68\n"
"s_nop 1\n"
"v_permlane32_swap_b32 v68, v69\n"
"v_mfma_f32_32x32x16_bf16 v[32:47], v[168:171], v[52:55], v[32:47]\n"
"v_mov_b32 v164, 0\n"
"v_mov_b32 v165, 0\n"
"v_mov_b32 v166, 0\n"
"ds_read_b64_tr_b16 v[52:53], v106 offset:13312\n"
"ds_read_b64_tr_b16 v[54:55], v107 offset:13312\n"
"v_sub_f32 v20, v20, v68\n"
"v_sub_f32 v21, v21, v68\n"
"buffer_load_dwordx4 v119, s[24:27], s63 offen offset:3072 lds\n"
"s_waitcnt vmcnt(12)\n"
"s_barrier\n"
"s_cmp_lg_u32 s32, 0\n"
"s_cbranch_scc1 L_hp3\n"
"v_accvgpr_read_b32 v161, a5\n"
"v_accvgpr_read_b32 v160, a4\n"
"v_lshrrev_b32 v162, 1, v115\n"
"v_add_u32 v161, 1, v161\n"
"s_mov_b64 exec, s[76:77]\n"
"buffer_store_dwordx2 v[160:161], v162, s[8:11], s36 offen offset:512 sc1\n"
"s_mov_b64 exec, -1\n"
"v_accvgpr_write_b32 a129, v161\n"
"v_accvgpr_write_b32 a204, v160\n"
"L_hp3:\n"
"s_add_u32 s34, s44, 2097152\n"
"buffer_store_dwordx4 a[0:3], v115, s[8:11], s34 offen offset:0 sc1\n"
"s_waitcnt lgkmcnt(15)\n"
"v_mfma_f32_32x32x16_bf16 v[32:47], v[172:175], v[56:59], v[32:47]\n"
"ds_read_b64_tr_b16 v[56:57], v106 offset:14336\n"
"ds_read_b64_tr_b16 v[58:59], v107 offset:14336\n"
"v_sub_f32 v22, v22, v68\n"
"v_sub_f32 v23, v23, v68\n"
"v_sub_f32 v64, v64, v69\n"
"s_add_u32 s34, s44, 2097152\n"
"buffer_store_dwordx4 a[0:3], v115, s[8:11], s34 offen offset:1024 sc1\n"
"v_dot2c_f32_bf16 v92, s52, v128\n"
"v_dot2c_f32_bf16 v164, s52, v129\n"
"v_dot2c_f32_bf16 v165, s52, v130\n"
"v_dot2c_f32_bf16 v166, s52, v131\n"
"v_dot2c_f32_bf16 v92, s52, v132\n"
"v_dot2c_f32_bf16 v164, s52, v133\n"
"v_dot2c_f32_bf16 v165, s52, v134\n"
"v_dot2c_f32_bf16 v166, s52, v135\n"
"v_dot2c_f32_bf16 v92, s52, v136\n"
"v_dot2c_f32_bf16 v164, s52, v137\n"
"s_waitcnt lgkmcnt(15)\n"
"v_dot2c_f32_bf16 v165, s52, v138\n"
"v_dot2c_f32_bf16 v166, s52, v139\n"
"v_dot2c_f32_bf16 v92, s52, v140\n"
"v_dot2c_f32_bf16 v164, s52, v141\n"
"s_waitcnt lgkmcnt(14)\n"
"v_dot2c_f32_bf16 v165, s52, v142\n"
"v_dot2c_f32_bf16 v166, s52, v143\n"
"v_sub_f32 v65, v65, v69\n"
"v_sub_f32 v66, v66, v69\n"
"s_add_u32 m0, s48, 36864\n"
"v_add_u32 v119, 16384, v119\n"
"buffer_load_dwordx4 v119, s[24:27], 0 offen offset:0 lds\n"
"s_cmp_eq_u32 s61, 3\n"
"s_cselect_b32 s34, 2097152, 0\n"
"s_add_u32 s34, s34, s46\n"
"buffer_store_dwordx4 a[0:3], v115, s[8:11], s34 offen offset:0 sc1\n"
"buffer_store_dwordx4 a[0:3], v115, s[8:11], s34 offen offset:1024 sc1\n"
"s_waitcnt lgkmcnt(12)\n"
"v_mfma_f32_32x32x16_bf16 v[32:47], v[176:179], v[48:51], v[32:47]\n"
"ds_read_b64_tr_b16 v[48:49], v106 offset:15360\n"
"ds_read_b64_tr_b16 v[50:51], v107 offset:15360\n"
"v_sub_f32 v67, v67, v69\n"
"v_exp_f32 v20, v20\n"
"v_exp_f32 v21, v21\n"
"buffer_load_dwordx4 v119, s[24:27], s55 offen offset:1024 lds\n"
"v_bfe_u32 v168, v115, 4, 5\n"
"v_lshrrev_b32 v169, 9, v115\n"
"v_lshlrev_b32 v168, 5, v168\n"
"v_lshl_add_u32 v168, v169, 2, v168\n"
"s_cmp_eq_u32 s32, 0\n"
"s_cselect_b32 s35, 0, 1073741824\n"
"v_or_b32 v168, s35, v168\n"
"s_add_u32 s34, s47, 65536\n"
"buffer_store_dword a0, v168, s[8:11], s34 offen sc1\n"
"s_waitcnt lgkmcnt(13)\n"
"v_dot2c_f32_bf16 v92, s52, v144\n"
"v_dot2c_f32_bf16 v164, s52, v145\n"
"s_waitcnt lgkmcnt(12)\n"
"v_dot2c_f32_bf16 v165, s52, v146\n"
"v_dot2c_f32_bf16 v166, s52, v147\n"
"s_waitcnt lgkmcnt(11)\n"
"v_dot2c_f32_bf16 v92, s52, v148\n"
"v_dot2c_f32_bf16 v164, s52, v149\n"
"s_waitcnt lgkmcnt(10)\n"
"v_dot2c_f32_bf16 v165, s52, v150\n"
"v_dot2c_f32_bf16 v166, s52, v151\n"
"s_waitcnt lgkmcnt(9)\n"
"v_dot2c_f32_bf16 v92, s52, v152\n"
"v_dot2c_f32_bf16 v164, s52, v153\n"
"s_waitcnt lgkmcnt(8)\n"
"v_dot2c_f32_bf16 v165, s52, v154\n"
"v_dot2c_f32_bf16 v166, s52, v155\n"
"s_waitcnt lgkmcnt(7)\n"
"v_dot2c_f32_bf16 v92, s52, v156\n"
"v_dot2c_f32_bf16 v164, s52, v157\n"
"s_waitcnt lgkmcnt(6)\n"
"v_dot2c_f32_bf16 v165, s52, v158\n"
"v_dot2c_f32_bf16 v166, s52, v159\n"
"s_nop 1\n"
"v_add_f32 v164, v164, v165\n"
"v_add_f32 v92, v92, v166\n"
"v_add_f32 v92, v92, v164\n"
"v_exp_f32 v22, v22\n"
"v_exp_f32 v23, v23\n"
"s_waitcnt lgkmcnt(4)\n"
"v_mfma_f32_32x32x16_bf16 v[32:47], v[180:183], v[52:55], v[32:47]\n"
"ds_read_b64_tr_b16 v[52:53], v106 offset:16384\n"
"ds_read_b64_tr_b16 v[54:55], v107 offset:16384\n"
"v_mov_b32 v128, v92\n"
"v_bfe_u32 v131, v115, 4, 5\n"
"s_cmp_lt_u32 s32, 2\n"
"s_cbranch_scc0 L_hw7\n"
"s_and_b32 s34, s32, 1\n"
"s_lshl_b32 s34, s34, 10\n"
"v_add_u32 v172, s34, v94\n"
"ds_write_b128 v172, v[88:91] offset:2048\n"
"L_hw7:\n"
"v_exp_f32 v64, v64\n"
"v_exp_f32 v65, v65\n"
"v_exp_f32 v66, v66\n"
"ds_read_b128 v[160:163], v116 offset:58368\n"
"ds_read_b128 v[164:167], v116 offset:59392\n"
"ds_read_b128 v[168:171], v116 offset:60416\n"
"buffer_load_dwordx4 v119, s[24:27], s56 offen offset:2048 lds\n"
"s_waitcnt lgkmcnt(7)\n"
"v_mfma_f32_32x32x16_bf16 v[32:47], v[184:187], v[56:59], v[32:47]\n"
"ds_read_b64_tr_b16 v[56:57], v106 offset:17408\n"
"ds_read_b64_tr_b16 v[58:59], v107 offset:17408\n"
"v_permlane32_swap_b32 v92, v128\n"
"v_exp_f32 v67, v67\n"
"v_add_f32 v68, v20, v21\n"
"s_waitcnt lgkmcnt(7)\n"
"v_mfma_f32_32x32x16_bf16 v[32:47], v[188:191], v[48:51], v[32:47]\n"
"ds_read_b64_tr_b16 v[48:49], v106 offset:18432\n"
"ds_read_b64_tr_b16 v[50:51], v107 offset:18432\n"
"v_add_f32 v92, v92, v128\n"
"s_lshl_b32 s34, s32, 2\n"
"s_add_u32 s34, s34, 159744\n"
"v_lshl_add_u32 v132, v131, 4, s34\n"
"v_add_f32 v70, v22, v23\n"
"v_add_f32 v69, v64, v65\n"
"v_add_f32 v71, v66, v67\n"
"buffer_load_dwordx4 v119, s[24:27], s63 offen offset:3072 lds\n"
"s_waitcnt lgkmcnt(7)\n"
"v_mfma_f32_32x32x16_bf16 v[32:47], v[192:195], v[52:55], v[32:47]\n"
"ds_read_b64_tr_b16 v[52:53], v106 offset:19456\n"
"ds_read_b64_tr_b16 v[54:55], v107 offset:19456\n"
"ds_write_b32 v132, v92 offset:1024\n"
"v_add_f32 v68, v68, v70\n"
"v_add_f32 v69, v69, v71\n"
"s_waitcnt lgkmcnt(5)\n"
"v_mfma_f32_32x32x16_bf16 v[32:47], v[196:199], v[56:59], v[32:47]\n"
"ds_read_b64_tr_b16 v[56:57], v106 offset:20480\n"
"ds_read_b64_tr_b16 v[58:59], v107 offset:20480\n"
"s_waitcnt lgkmcnt(0)\n"
"s_barrier\n"
"v_mov_b32 v180, 0\n"
"v_mov_b32 v181, 0\n"
"v_mov_b32 v192, 0\n"
"v_mov_b32 v193, 0\n"
"v_mfma_f32_32x32x16_bf16 v[32:47], v[200:203], v[48:51], v[32:47]\n"
"ds_read_b64_tr_b16 v[48:49], v106 offset:21504\n"
"ds_read_b64_tr_b16 v[50:51], v107 offset:21504\n"
"v_bfe_u32 v131, v115, 4, 5\n"
"s_mov_b32 s34, 2\n"
"s_lshl_b32 s34, s34, 9\n"
"s_add_u32 s34, s34, 159744\n"
"v_lshl_add_u32 v132, v131, 4, s34\n"
"ds_read_b128 v[134:137], v132\n"
"s_or_b32 s34, s32, 2\n"
"s_lshl_b32 s34, s34, 9\n"
"s_add_u32 s34, s34, 147456\n"
"v_lshl_add_u32 v132, v131, 4, s34\n"
"v_mul_f32 v20, v20, v60\n"
"v_mul_f32 v21, v21, v61\n"
"v_permlane32_swap_b32 v68, v69\n"
"s_add_u32 m0, s48, 98304\n"
"v_add_u32 v119, 245760, v119\n"
"buffer_load_dwordx4 v119, s[24:27], 0 offen offset:0 lds\n"
"v_mfma_f32_32x32x16_bf16 v[32:47], v[204:207], v[52:55], v[32:47]\n"
"ds_read_b64_tr_b16 v[52:53], v106 offset:22528\n"
"ds_read_b64_tr_b16 v[54:55], v107 offset:22528\n"
"s_waitcnt lgkmcnt(2)\n"
"v_add_f32 v134, v134, v135\n"
"v_add_f32 v136, v136, v137\n"
"v_add_f32 v134, v134, v136\n"
"v_cvt_pk_bf16_f32 v135, v134, v134\n"
"s_and_b32 s34, s32, 1\n"
"s_lshl_b32 s34, s34, 10\n"
"v_add_u32 v148, s34, v94\n"
"ds_read_b128 v[148:151], v148 offset:2048\n"
"v_add_f32 v68, v68, v69\n"
"v_rcp_f32 v68, v68\n"
"v_mul_f32 v22, v22, v62\n"
"buffer_load_dwordx4 v119, s[24:27], s55 offen offset:1024 lds\n"
"v_mfma_f32_32x32x16_bf16 v[32:47], v[208:211], v[56:59], v[32:47]\n"
"ds_read_b64_tr_b16 v[56:57], v106 offset:23552\n"
"ds_read_b64_tr_b16 v[58:59], v107 offset:23552\n"
"v_lshlrev_b32 v135, 16, v135\n"
"v_sub_f32 v136, v134, v135\n"
"v_cvt_pk_bf16_f32 v134, v135, v136\n"
"v_mov_b32 v69, v68\n"
"v_mul_f32 v23, v23, v63\n"
"v_mul_f32 v64, v64, v60\n"
"v_permlane32_swap_b32 v68, v69\n"
"v_mfma_f32_32x32x16_bf16 v[32:47], v[212:215], v[48:51], v[32:47]\n"
"ds_read_b64_tr_b16 v[48:49], v106 offset:24576\n"
"ds_read_b64_tr_b16 v[50:51], v107 offset:24576\n"
"v_and_b32 v137, 0xffff0000, v134\n"
"v_sub_f32 v136, v136, v137\n"
"v_mov_b32 v137, 0\n"
"v_cvt_pk_bf16_f32 v135, v136, v137\n"
"v_mul_f32 v65, v65, v61\n"
"v_mul_f32 v66, v66, v62\n"
"v_mul_f32 v67, v67, v63\n"
"buffer_load_dwordx4 v119, s[24:27], s56 offen offset:2048 lds\n"
"s_waitcnt lgkmcnt(5)\n"
"v_mfma_f32_32x32x16_bf16 v[32:47], v[216:219], v[52:55], v[32:47]\n"
"ds_read_b64_tr_b16 v[52:53], v106 offset:25600\n"
"ds_read_b64_tr_b16 v[54:55], v107 offset:25600\n"
"v_mov_b32 v136, 0\n"
"ds_write_b128 v132, v[134:137]\n"
"s_waitcnt lgkmcnt(0)\n"
"s_barrier\n"
"v_mul_f32 v20, v20, v68\n"
"v_mul_f32 v21, v21, v68\n"
"v_mul_f32 v22, v22, v68\n"
"buffer_load_dwordx4 v119, s[24:27], s63 offen offset:3072 lds\n"
"v_mfma_f32_32x32x16_bf16 v[32:47], v[220:223], v[56:59], v[32:47]\n"
"ds_read_b64_tr_b16 v[56:57], v106 offset:26624\n"
"ds_read_b64_tr_b16 v[58:59], v107 offset:26624\n"
"ds_read_b128 v[196:199], v94 offset:1024\n"
"v_and_b32 v139, 0xfffffcff, v94\n"
"v_mul_f32 v23, v23, v68\n"
"v_cvt_pk_bf16_f32 v20, v20, v21\n"
"v_cvt_pk_bf16_f32 v21, v22, v23\n"
"v_mfma_f32_32x32x16_bf16 v[32:47], v[224:227], v[48:51], v[32:47]\n"
"ds_read_b64_tr_b16 v[48:49], v106 offset:27648\n"
"ds_read_b64_tr_b16 v[50:51], v107 offset:27648\n"
"v_add_u32 v139, s53, v139\n"
"ds_read_b128 v[140:143], v139\n"
"v_add_f32 v88, v88, v148\n"
"v_add_f32 v89, v89, v149\n"
"v_and_b32 v172, v20, v162\n"
"v_and_b32 v173, v21, v163\n"
"v_and_b32 v174, v20, v164\n"
"v_and_b32 v175, v21, v165\n"
"s_add_u32 m0, s48, 102400\n"
"v_add_u32 v119, 16384, v119\n"
"buffer_load_dwordx4 v119, s[24:27], 0 offen offset:0 lds\n"
"v_mfma_f32_32x32x16_bf16 v[32:47], v[228:231], v[52:55], v[32:47]\n"
"ds_read_b64_tr_b16 v[52:53], v106 offset:28672\n"
"ds_read_b64_tr_b16 v[54:55], v107 offset:28672\n"
"v_add_f32 v90, v90, v150\n"
"v_add_f32 v91, v91, v151\n"
"v_and_b32 v176, v20, v166\n"
"v_and_b32 v177, v21, v167\n"
"v_and_b32 v178, v20, v168\n"
"s_waitcnt lgkmcnt(6)\n"
"v_mfma_f32_32x32x16_bf16 v[32:47], v[232:235], v[56:59], v[32:47]\n"
"ds_read_b64_tr_b16 v[56:57], v106 offset:29696\n"
"ds_read_b64_tr_b16 v[58:59], v107 offset:29696\n"
"v_and_b32 v179, v21, v169\n"
"v_and_b32 v182, v20, v160\n"
"v_and_b32 v183, v21, v161\n"
"buffer_load_dwordx4 v119, s[24:27], s55 offen offset:1024 lds\n"
"s_waitcnt lgkmcnt(5)\n"
"v_mfma_f32_32x32x16_bf16 v[32:47], v[236:239], v[48:51], v[32:47]\n"
"ds_read_b64_tr_b16 v[48:49], v106 offset:30720\n"
"ds_read_b64_tr_b16 v[50:51], v107 offset:30720\n"
"s_waitcnt lgkmcnt(6)\n"
"v_mfma_f32_16x16x32_bf16 v[88:91], v[140:143], v[100:103], v[88:91]\n"
"v_mul_f32 v64, v64, v69\n"
"v_mul_f32 v65, v65, v69\n"
"v_mul_f32 v66, v66, v69\n"
"buffer_load_dwordx4 v119, s[24:27], s56 offen offset:2048 lds\n"
"s_cmp_lg_u32 s32, 0\n"
"s_cbranch_scc1 L_hi11\n"
"v_lshrrev_b32 v209, 1, v115\n"
"v_accvgpr_read_b32 v204, a128\n"
"v_accvgpr_read_b32 v205, a129\n"
"v_accvgpr_read_b32 v206, a130\n"
"v_accvgpr_read_b32 v207, a131\n"
"v_accvgpr_read_b32 v210, a204\n"
"s_mov_b64 exec, s[74:75]\n"
"buffer_load_dwordx2 v[200:201], v206, s[8:11], 0 offen sc1\n"
"s_mov_b64 exec, s[76:77]\n"
"buffer_load_dwordx2 v[202:203], v207, s[8:11], 0 offen sc1\n"
"s_mov_b64 exec, -1\n"
"L_hi11:\n"
"s_waitcnt lgkmcnt(4)\n"
"v_mfma_f32_32x32x16_bf16 v[32:47], v[240:243], v[52:55], v[32:47]\n"
"ds_read_b64_tr_b16 v[52:53], v106 offset:31744\n"
"ds_read_b64_tr_b16 v[54:55], v107 offset:31744\n"
"v_mul_f32 v67, v67, v69\n"
"v_cvt_pk_bf16_f32 v64, v64, v65\n"
"v_cvt_pk_bf16_f32 v65, v66, v67\n"
"v_and_b32 v184, v64, v162\n"
"s_waitcnt lgkmcnt(4)\n"
"v_mfma_f32_32x32x16_bf16 v[32:47], v[244:247], v[56:59], v[32:47]\n"
"v_cvt_pk_bf16_f32 v88, v88, v89\n"
"v_cvt_pk_bf16_f32 v89, v90, v91\n"
"s_cmp_lt_u32 s32, 2\n"
"s_cbranch_scc1 L_hd9\n"
"ds_write_b16 v121, v88\n"
"ds_write_b16_d16_hi v121, v88 offset:16\n"
"ds_write_b16 v121, v89 offset:32\n"
"ds_write_b16_d16_hi v121, v89 offset:48\n"
"L_hd9:\n"
"v_and_b32 v185, v65, v163\n"
"v_and_b32 v186, v64, v164\n"
"v_and_b32 v187, v65, v165\n"
"buffer_load_dwordx4 v119, s[24:27], s63 offen offset:3072 lds\n"
"s_waitcnt lgkmcnt(2)\n"
"v_mfma_f32_32x32x16_bf16 v[32:47], v[248:251], v[48:51], v[32:47]\n"
"v_and_b32 v188, v64, v166\n"
"v_and_b32 v189, v65, v167\n"
"v_and_b32 v190, v64, v168\n"
"s_waitcnt lgkmcnt(0)\n"
"v_mfma_f32_32x32x16_bf16 v[32:47], v[252:255], v[52:55], v[32:47]\n"
"v_and_b32 v191, v65, v169\n"
"v_and_b32 v194, v64, v160\n"
"v_and_b32 v195, v65, v161\n"
"v_mfma_f32_32x32x16_bf16 v[32:47], v[96:99], v[196:199], v[32:47]\n"
"s_nop 11\n"
"v_cvt_pk_bf16_f32 v40, v40, v41\n"
"v_cvt_pk_bf16_f32 v41, v42, v43\n"
"v_cvt_pk_bf16_f32 v42, v44, v45\n"
"v_cvt_pk_bf16_f32 v43, v46, v47\n"
"ds_write_b128 v117, v[40:43] offset:37888\n"
"v_cvt_pk_bf16_f32 v32, v32, v33\n"
"v_mfma_f32_32x32x16_bf16 v[0:15], v[40:43], v[176:179], 0\n"
"v_cvt_pk_bf16_f32 v33, v34, v35\n"
"v_cvt_pk_bf16_f32 v34, v36, v37\n"
"v_cvt_pk_bf16_f32 v35, v38, v39\n"
"v_mfma_f32_32x32x16_bf16 v[16:31], v[40:43], v[188:191], 0\n"
"s_waitcnt vmcnt(8)\n"
"s_waitcnt lgkmcnt(0)\n"
"s_barrier\n"
"ds_read_b128 v[44:47], v117 offset:34816\n"
"v_mfma_f32_32x32x16_bf16 v[0:15], v[32:35], v[172:175], v[0:15]\n"
"v_mfma_f32_32x32x16_bf16 v[16:31], v[32:35], v[184:187], v[16:31]\n"
"s_waitcnt lgkmcnt(0)\n"
"v_mfma_f32_32x32x16_bf16 v[0:15], v[44:47], v[180:183], v[0:15]\n"
"v_mfma_f32_32x32x16_bf16 v[16:31], v[44:47], v[192:195], v[16:31]\n"
"s_nop 10\n"
"v_cvt_pk_bf16_f32 v48, v0, v1\n"
"v_cvt_pk_bf16_f32 v49, v2, v3\n"
"v_cvt_pk_bf16_f32 v50, v4, v5\n"
"v_cvt_pk_bf16_f32 v51, v6, v7\n"
"v_min_u32 v48, -2, v48\n"
"buffer_store_dwordx4 v[48:51], v115, s[8:11], s44 offen sc1\n"
"v_cvt_pk_bf16_f32 v52, v24, v25\n"
"v_cvt_pk_bf16_f32 v53, v26, v27\n"
"v_cvt_pk_bf16_f32 v54, v28, v29\n"
"v_cvt_pk_bf16_f32 v55, v30, v31\n"
"v_min_u32 v52, -2, v52\n"
"buffer_store_dwordx4 v[52:55], v115, s[8:11], s44 offen offset:1024 sc1\n"
"s_add_u32 m0, s48, 65536\n"
"v_add_u32 v119, 4294819840, v119\n"
"buffer_load_dwordx4 v119, s[24:27], 0 offen offset:0 lds\n"
"buffer_load_dwordx4 v119, s[24:27], s55 offen offset:1024 lds\n"
"buffer_load_dwordx4 v119, s[24:27], s56 offen offset:2048 lds\n"
"buffer_load_dwordx4 v119, s[24:27], s63 offen offset:3072 lds\n"
"s_add_u32 m0, s48, 69632\n"
"v_add_u32 v119, 16384, v119\n"
"buffer_load_dwordx4 v119, s[24:27], 0 offen offset:0 lds\n"
"buffer_load_dwordx4 v119, s[24:27], s55 offen offset:1024 lds\n"
"buffer_load_dwordx4 v119, s[24:27], s56 offen offset:2048 lds\n"
"buffer_load_dwordx4 v119, s[24:27], s63 offen offset:3072 lds\n"
"v_lshrrev_b32 v58, 9, v115\n"
"v_bfe_u32 v59, v115, 6, 2\n"
"v_lshl_add_u32 v58, v58, 2, v59\n"
"v_lshlrev_b32 v58, 8, v58\n"
"v_xor_b32 v59, s32, v59\n"
"v_lshl_add_u32 v58, v59, 6, v58\n"
"v_bfe_u32 v59, v115, 8, 1\n"
"v_lshl_add_u32 v58, v59, 5, v58\n"
"v_bfe_u32 v59, v115, 4, 2\n"
"v_lshl_add_u32 v58, v59, 3, v58\n"
"v_add_u32 v59, 0x10000, v58\n"
"ds_read_b64_tr_b16 a[0:1], v58 offset:0\n"
"ds_read_b64_tr_b16 a[2:3], v58 offset:2048\n"
"ds_read_b64_tr_b16 a[4:5], v58 offset:4096\n"
"ds_read_b64_tr_b16 a[6:7], v58 offset:6144\n"
"ds_read_b64_tr_b16 a[8:9], v58 offset:8192\n"
"ds_read_b64_tr_b16 a[10:11], v58 offset:10240\n"
"ds_read_b64_tr_b16 a[12:13], v58 offset:12288\n"
"ds_read_b64_tr_b16 a[14:15], v58 offset:14336\n"
"ds_read_b64_tr_b16 a[16:17], v58 offset:16384\n"
"ds_read_b64_tr_b16 a[18:19], v58 offset:18432\n"
"ds_read_b64_tr_b16 a[20:21], v58 offset:20480\n"
"ds_read_b64_tr_b16 a[22:23], v58 offset:22528\n"
"ds_read_b64_tr_b16 a[24:25], v58 offset:24576\n"
"ds_read_b64_tr_b16 a[26:27], v58 offset:26624\n"
"ds_read_b64_tr_b16 a[28:29], v58 offset:28672\n"
"ds_read_b64_tr_b16 a[30:31], v58 offset:30720\n"
"ds_read_b64_tr_b16 a[32:33], v58 offset:32768\n"
"ds_read_b64_tr_b16 a[34:35], v58 offset:34816\n"
"ds_read_b64_tr_b16 a[36:37], v58 offset:36864\n"
"ds_read_b64_tr_b16 a[38:39], v58 offset:38912\n"
"ds_read_b64_tr_b16 a[40:41], v58 offset:40960\n"
"ds_read_b64_tr_b16 a[42:43], v58 offset:43008\n"
"ds_read_b64_tr_b16 a[44:45], v58 offset:45056\n"
"ds_read_b64_tr_b16 a[46:47], v58 offset:47104\n"
"ds_read_b64_tr_b16 a[48:49], v58 offset:49152\n"
"ds_read_b64_tr_b16 a[50:51], v58 offset:51200\n"
"ds_read_b64_tr_b16 a[52:53], v58 offset:53248\n"
"ds_read_b64_tr_b16 a[54:55], v58 offset:55296\n"
"ds_read_b64_tr_b16 a[56:57], v58 offset:57344\n"
"ds_read_b64_tr_b16 a[58:59], v58 offset:59392\n"
"ds_read_b64_tr_b16 a[60:61], v58 offset:61440\n"
"ds_read_b64_tr_b16 a[62:63], v58 offset:63488\n"
"v_accvgpr_read_b32 v104, a180\n"
"v_accvgpr_read_b32 v105, a181\n"
"v_accvgpr_read_b32 v106, a182\n"
"v_accvgpr_read_b32 v107, a183\n"
"v_accvgpr_read_b32 v108, a184\n"
"v_accvgpr_read_b32 v109, a185\n"
"v_accvgpr_read_b32 v110, a186\n"
"v_accvgpr_read_b32 v111, a187\n"
"s_mov_b32 s24, s22\n"
"s_mov_b32 s25, s23\n"
"v_lshlrev_b32 v32, 16, v104\n"
"v_and_b32 v33, 0xffff0000, v104\n"
"v_lshlrev_b32 v34, 16, v105\n"
"v_and_b32 v35, 0xffff0000, v105\n"
"v_lshlrev_b32 v36, 16, v106\n"
"v_and_b32 v37, 0xffff0000, v106\n"
"v_lshlrev_b32 v38, 16, v107\n"
"v_and_b32 v39, 0xffff0000, v107\n"
"v_lshlrev_b32 v40, 16, v108\n"
"v_and_b32 v41, 0xffff0000, v108\n"
"v_lshlrev_b32 v42, 16, v109\n"
"v_and_b32 v43, 0xffff0000, v109\n"
"v_lshlrev_b32 v44, 16, v110\n"
"v_and_b32 v45, 0xffff0000, v110\n"
"v_lshlrev_b32 v46, 16, v111\n"
"v_and_b32 v47, 0xffff0000, v111\n"
"v_lshrrev_b32 v56, 2, v115\n"
"v_and_b32 v56, 0x30, v56\n"
"v_lshl_add_u32 v56, v115, 2, v56\n"
"v_add_u32 v56, s51, v56\n"
"s_waitcnt vmcnt(10)\n"
"s_waitcnt lgkmcnt(0)\n"
"s_barrier\n"
"ds_read_b64_tr_b16 a[96:97], v59 offset:32768\n"
"ds_read_b64_tr_b16 a[98:99], v59 offset:34816\n"
"ds_read_b64_tr_b16 a[100:101], v59 offset:36864\n"
"ds_read_b64_tr_b16 a[102:103], v59 offset:38912\n"
"ds_read_b64_tr_b16 a[104:105], v59 offset:40960\n"
"ds_read_b64_tr_b16 a[106:107], v59 offset:43008\n"
"ds_read_b64_tr_b16 a[108:109], v59 offset:45056\n"
"ds_read_b64_tr_b16 a[110:111], v59 offset:47104\n"
"ds_read_b64_tr_b16 a[112:113], v59 offset:49152\n"
"ds_read_b64_tr_b16 a[114:115], v59 offset:51200\n"
"ds_read_b64_tr_b16 a[116:117], v59 offset:53248\n"
"ds_read_b64_tr_b16 a[118:119], v59 offset:55296\n"
"ds_read_b64_tr_b16 a[120:121], v59 offset:57344\n"
"ds_read_b64_tr_b16 a[122:123], v59 offset:59392\n"
"ds_read_b64_tr_b16 a[124:125], v59 offset:61440\n"
"ds_read_b64_tr_b16 a[126:127], v59 offset:63488\n"
"s_cmp_lg_u32 s32, 0\n"
"s_cbranch_scc1 L_hc12_done\n"
"s_mov_b64 exec, s[76:77]\n"
"v_cmp_ne_u32 s[34:35], v205, v203\n"
"v_cmp_ne_u32 vcc, v202, v210\n"
"s_and_b64 exec, vcc, s[34:35]\n"
"s_cbranch_execz L_hc12a_noack\n"
"v_mov_b32 v203, v205\n"
"v_mov_b32 v210, v202\n"
"v_accvgpr_write_b32 a204, v202\n"
"buffer_store_dwordx2 v[202:203], v209, s[8:11], s36 offen offset:512 sc1\n"
"L_hc12a_noack:\n"
"s_mov_b64 exec, s[74:75]\n"
"v_cmp_ne_u32 vcc, v200, v204\n"
"s_mov_b64 exec, -1\n"
"s_cmp_eq_u64 vcc, 0\n"
"s_cbranch_scc0 L_hc12_loop\n"
"L_hc12_done:\n"
"s_waitcnt lgkmcnt(0)\n"
"s_barrier\n"
"s_waitcnt vmcnt(8)\n"
"s_mov_b32 m0, s51\n"
"s_nop 0\n"
"buffer_load_dwordx4 v115, s[8:11], s45 offen offset:0 sc1 lds\n"
"buffer_load_dwordx4 v115, s[8:11], s45 offen offset:1024 sc1 lds\n"
"buffer_load_dwordx4 v115, s[8:11], s45 offen offset:2048 sc1 lds\n"
"buffer_load_dwordx4 v115, s[8:11], s45 offen offset:3072 sc1 lds\n"
"s_add_u32 m0, s51, 16384\n"
"s_add_u32 s35, s45, 16384\n"
"buffer_load_dwordx4 v115, s[8:11], s35 offen offset:0 sc1 lds\n"
"buffer_load_dwordx4 v115, s[8:11], s35 offen offset:1024 sc1 lds\n"
"buffer_load_dwordx4 v115, s[8:11], s35 offen offset:2048 sc1 lds\n"
"buffer_load_dwordx4 v115, s[8:11], s35 offen offset:3072 sc1 lds\n"
"s_add_u32 m0, s48, 0\n"
"v_add_u32 v119, 0, v126\n"
"buffer_load_dwordx4 v119, s[8:11], 0 offen offset:0 sc1 lds\n"
"buffer_load_dwordx4 v119, s[8:11], 0 offen offset:1024 sc1 lds\n"
"buffer_load_dwordx4 v119, s[8:11], 0 offen offset:2048 sc1 lds\n"
"buffer_load_dwordx4 v119, s[8:11], 0 offen offset:3072 sc1 lds\n"
"s_waitcnt vmcnt(12)\n"
"s_waitcnt lgkmcnt(0)\n"
"s_barrier\n"
"ds_read_b64_tr_b16 a[64:65], v59 offset:0\n"
"ds_read_b64_tr_b16 a[66:67], v59 offset:2048\n"
"ds_read_b64_tr_b16 a[68:69], v59 offset:4096\n"
"ds_read_b64_tr_b16 a[70:71], v59 offset:6144\n"
"ds_read_b64_tr_b16 a[72:73], v59 offset:8192\n"
"ds_read_b64_tr_b16 a[74:75], v59 offset:10240\n"
"ds_read_b64_tr_b16 a[76:77], v59 offset:12288\n"
"ds_read_b64_tr_b16 a[78:79], v59 offset:14336\n"
"ds_read_b64_tr_b16 a[80:81], v59 offset:16384\n"
"ds_read_b64_tr_b16 a[82:83], v59 offset:18432\n"
"ds_read_b64_tr_b16 a[84:85], v59 offset:20480\n"
"ds_read_b64_tr_b16 a[86:87], v59 offset:22528\n"
"ds_read_b64_tr_b16 a[88:89], v59 offset:24576\n"
"ds_read_b64_tr_b16 a[90:91], v59 offset:26624\n"
"ds_read_b64_tr_b16 a[92:93], v59 offset:28672\n"
"ds_read_b64_tr_b16 a[94:95], v59 offset:30720\n"
"s_add_u32 m0, s48, 4096\n"
"v_add_u32 v119, 4096, v126\n"
"buffer_load_dwordx4 v119, s[8:11], 0 offen offset:0 sc1 lds\n"
"buffer_load_dwordx4 v119, s[8:11], 0 offen offset:1024 sc1 lds\n"
"buffer_load_dwordx4 v119, s[8:11], 0 offen offset:2048 sc1 lds\n"
"buffer_load_dwordx4 v119, s[8:11], 0 offen offset:3072 sc1 lds\n"
"s_add_u32 m0, s48, 32768\n"
"v_add_u32 v119, 32768, v126\n"
"buffer_load_dwordx4 v119, s[8:11], 0 offen offset:0 sc1 lds\n"
"buffer_load_dwordx4 v119, s[8:11], 0 offen offset:1024 sc1 lds\n"
"v_lshrrev_b32 v12, 9, v115\n"
"v_bfe_u32 v13, v115, 8, 1\n"
"v_lshl_add_u32 v12, v12, 1, v13\n"
"v_bfe_u32 v13, v115, 7, 1\n"
"v_and_b32 v14, 0x70, v115\n"
"v_lshl_add_u32 v14, v13, 9, v14\n"
"v_lshl_add_u32 v12, v12, 10, v14\n"
"s_cmp_gt_u32 s59, 0\n"
"s_cselect_b32 s34, 384, 1073741824\n"
"s_cselect_b32 s35, 8, 99\n"
"v_add_u32 v0, s34, v12\n"
"v_bfe_u32 v12, v115, 4, 5\n"
"v_lshrrev_b32 v13, 9, v115\n"
"v_lshlrev_b32 v12, 5, v12\n"
"v_lshl_add_u32 v1, v13, 4, v12\n"
"v_bfe_u32 v12, v115, 4, 4\n"
"v_lshrrev_b32 v13, 8, v115\n"
"v_cmp_lt_u32 vcc, s35, v12\n"
"v_lshlrev_b32 v12, 5, v12\n"
"v_lshl_add_u32 v12, v13, 3, v12\n"
"v_add_u32 v12, 512, v12\n"
"v_mov_b32 v13, 1073741824\n"
"v_cndmask_b32 v2, v13, v12\n"
"v_add_u32 v3, s50, v115\n"
"v_bfe_u32 v4, v115, 4, 5\n"
"v_lshrrev_b32 v5, 9, v115\n"
"v_lshlrev_b32 v4, 5, v4\n"
"v_lshl_add_u32 v4, v5, 2, v4\n"
"s_cmp_eq_u32 s32, 0\n"
"s_cselect_b32 s35, 0, 1073741824\n"
"v_or_b32 v4, s35, v4\n"
"v_lshrrev_b32 v5, 2, v115\n"
"v_add_u32 v5, 161792, v5\n"
"s_lshl_b32 s34, s32, 8\n"
"v_add_u32 v6, s34, v5\n"
"v_mov_b32 v8, 0\n"
"v_mov_b32 v9, 0\n"
"v_xor_b32 v94, 2048, v94\n"
"s_add_u32 s44, s44, 2097152\n"
"s_add_u32 s49, s44, 2097152\n"
"s_add_u32 s54, s46, 4194304\n"
"s_add_u32 s60, s47, 131072\n"
"s_mov_b32 s33, 1\n"
"s_cmp_eq_u32 s61, 3\n"
"s_cselect_b32 s54, 2, 1\n"
"s_lshl_b32 s54, s54, 21\n"
"s_add_u32 s54, s54, s46\n"
"s_cmp_lg_u32 s32, 0\n"
"s_cbranch_scc1 L_fs13\n"
"s_mov_b64 exec, s[74:75]\n"
"buffer_store_dword v201, v209, s[8:11], s36 offen offset:4 sc1\n"
"s_mov_b64 exec, -1\n"
"L_fs13:\n"
"s_waitcnt vmcnt(14)\n"
"ds_read_b32 v61, v56 offset:0\n"
"s_waitcnt lgkmcnt(0)\n"
"v_cmp_eq_u32 vcc, -1, v61\n"
"s_cbranch_vccnz L_os0\n"
"L_or0:\n"
"s_waitcnt lgkmcnt(0)\n"
"s_barrier\n"
"ds_read_b128 v[128:131], v116 offset:0\n"
"ds_read_b128 v[132:135], v116 offset:1024\n"
"ds_read_b128 v[136:139], v116 offset:2048\n"
"ds_read_b128 v[140:143], v116 offset:3072\n"
"v_mov_b32 v92, 0\n"
"s_waitcnt lgkmcnt(3)\n"
"v_mfma_f32_32x32x16_bf16 v[32:47], a[0:3], v[128:131], v[32:47]\n"
"ds_read_b128 v[144:147], v116 offset:4096\n"
"s_add_u32 m0, s48, 32768\n"
"v_add_u32 v119, 32768, v126\n"
"buffer_load_dwordx4 v119, s[8:11], 0 offen offset:2048 sc1 lds\n"
"s_waitcnt lgkmcnt(3)\n"
"v_mfma_f32_32x32x16_bf16 v[32:47], a[4:7], v[132:135], v[32:47]\n"
"ds_read_b128 v[148:151], v116 offset:5120\n"
"v_max3_u32 v92, v92, v128, v132\n"
"s_waitcnt lgkmcnt(3)\n"
"v_mfma_f32_32x32x16_bf16 v[32:47], a[8:11], v[136:139], v[32:47]\n"
"ds_read_b128 v[152:155], v116 offset:6144\n"
"s_waitcnt lgkmcnt(3)\n"
"v_mfma_f32_32x32x16_bf16 v[32:47], a[12:15], v[140:143], v[32:47]\n"
"ds_read_b128 v[156:159], v116 offset:7168\n"
"v_max3_u32 v92, v92, v136, v140\n"
"buffer_load_dwordx4 v119, s[8:11], 0 offen offset:3072 sc1 lds\n"
"s_waitcnt lgkmcnt(3)\n"
"v_mfma_f32_32x32x16_bf16 v[32:47], a[16:19], v[144:147], v[32:47]\n"
"ds_read_b128 v[160:163], v116 offset:8192\n"
"s_waitcnt lgkmcnt(3)\n"
"v_mfma_f32_32x32x16_bf16 v[32:47], a[20:23], v[148:151], v[32:47]\n"
"ds_read_b128 v[164:167], v116 offset:9216\n"
"v_max3_u32 v92, v92, v144, v148\n"
"s_waitcnt lgkmcnt(3)\n"
"v_mfma_f32_32x32x16_bf16 v[32:47], a[24:27], v[152:155], v[32:47]\n"
"ds_read_b128 v[168:171], v116 offset:10240\n"
"s_add_u32 m0, s48, 36864\n"
"v_add_u32 v119, 36864, v126\n"
"buffer_load_dwordx4 v119, s[8:11], 0 offen offset:0 sc1 lds\n"
"s_waitcnt lgkmcnt(3)\n"
"v_mfma_f32_32x32x16_bf16 v[32:47], a[28:31], v[156:159], v[32:47]\n"
"ds_read_b128 v[172:175], v116 offset:11264\n"
"v_max3_u32 v92, v92, v152, v156\n"
"s_waitcnt lgkmcnt(3)\n"
"v_mfma_f32_32x32x16_bf16 v[32:47], a[32:35], v[160:163], v[32:47]\n"
"ds_read_b128 v[176:179], v116 offset:12288\n"
"s_waitcnt lgkmcnt(3)\n"
"v_mfma_f32_32x32x16_bf16 v[32:47], a[36:39], v[164:167], v[32:47]\n"
"s_waitcnt vmcnt(13)\n"
"ds_read_b32 v61, v56 offset:16384\n"
"ds_read_b128 v[180:183], v116 offset:13312\n"
"v_max3_u32 v92, v92, v160, v164\n"
"buffer_load_dwordx4 v119, s[8:11], 0 offen offset:1024 sc1 lds\n"
"s_waitcnt lgkmcnt(4)\n"
"v_mfma_f32_32x32x16_bf16 v[32:47], a[40:43], v[168:171], v[32:47]\n"
"ds_read_b128 v[184:187], v116 offset:14336\n"
"s_waitcnt lgkmcnt(4)\n"
"v_mfma_f32_32x32x16_bf16 v[32:47], a[44:47], v[172:175], v[32:47]\n"
"s_waitcnt lgkmcnt(2)\n"
"v_cmp_eq_u32 vcc, -1, v61\n"
"s_cbranch_vccnz L_ot0\n"
"L_ou0:\n"
"s_barrier\n"
"ds_read_b128 v[188:191], v116 offset:15360\n"
"v_max3_u32 v92, v92, v168, v172\n"
"v_mfma_f32_32x32x16_bf16 v[32:47], a[48:51], v[176:179], v[32:47]\n"
"ds_read_b128 v[192:195], v116 offset:16384\n"
"buffer_load_dwordx4 v119, s[8:11], 0 offen offset:2048 sc1 lds\n"
"s_waitcnt lgkmcnt(3)\n"
"v_mfma_f32_32x32x16_bf16 v[32:47], a[52:55], v[180:183], v[32:47]\n"
"ds_read_b128 v[196:199], v116 offset:17408\n"
"v_max3_u32 v92, v92, v176, v180\n"
"s_waitcnt lgkmcnt(3)\n"
"v_mfma_f32_32x32x16_bf16 v[32:47], a[56:59], v[184:187], v[32:47]\n"
"ds_read_b128 v[200:203], v116 offset:18432\n"
"s_waitcnt lgkmcnt(3)\n"
"v_mfma_f32_32x32x16_bf16 v[32:47], a[60:63], v[188:191], v[32:47]\n"
"ds_read_b128 v[204:207], v116 offset:19456\n"
"v_max3_u32 v92, v92, v184, v188\n"
"buffer_load_dwordx4 v119, s[8:11], 0 offen offset:3072 sc1 lds\n"
"s_waitcnt lgkmcnt(3)\n"
"v_mfma_f32_32x32x16_bf16 v[32:47], a[64:67], v[192:195], v[32:47]\n"
"ds_read_b128 v[208:211], v116 offset:20480\n"
"s_waitcnt lgkmcnt(3)\n"
"v_mfma_f32_32x32x16_bf16 v[32:47], a[68:71], v[196:199], v[32:47]\n"
"ds_read_b128 v[212:215], v116 offset:21504\n"
"v_max3_u32 v92, v92, v192, v196\n"
"s_waitcnt lgkmcnt(3)\n"
"v_mfma_f32_32x32x16_bf16 v[32:47], a[72:75], v[200:203], v[32:47]\n"
"ds_read_b128 v[216:219], v116 offset:22528\n"
"s_add_u32 m0, s48, 65536\n"
"v_add_u32 v119, 65536, v126\n"
"buffer_load_dwordx4 v119, s[8:11], 0 offen offset:0 sc1 lds\n"
"s_waitcnt lgkmcnt(3)\n"
"v_mfma_f32_32x32x16_bf16 v[32:47], a[76:79], v[204:207], v[32:47]\n"
"ds_read_b128 v[220:223], v116 offset:23552\n"
"v_max3_u32 v92, v92, v200, v204\n"
"s_waitcnt lgkmcnt(3)\n"
"v_mfma_f32_32x32x16_bf16 v[32:47], a[80:83], v[208:211], v[32:47]\n"
"ds_read_b128 v[224:227], v116 offset:24576\n"
"s_waitcnt lgkmcnt(3)\n"
"v_mfma_f32_32x32x16_bf16 v[32:47], a[84:87], v[212:215], v[32:47]\n"
"ds_read_b128 v[228:231], v116 offset:25600\n"
"v_max3_u32 v92, v92, v208, v212\n"
"buffer_load_dwordx4 v119, s[8:11], 0 offen offset:1024 sc1 lds\n"
"s_waitcnt lgkmcnt(3)\n"
"v_mfma_f32_32x32x16_bf16 v[32:47], a[88:91], v[216:219], v[32:47]\n"
"ds_read_b128 v[232:235], v116 offset:26624\n"
"s_waitcnt lgkmcnt(3)\n"
"v_mfma_f32_32x32x16_bf16 v[32:47], a[92:95], v[220:223], v[32:47]\n"
"ds_read_b128 v[236:239], v116 offset:27648\n"
"v_max3_u32 v92, v92, v216, v220\n"
"s_waitcnt lgkmcnt(3)\n"
"v_mfma_f32_32x32x16_bf16 v[32:47], a[96:99], v[224:227], v[32:47]\n"
"ds_read_b128 v[240:243], v116 offset:28672\n"
"buffer_load_dwordx4 v119, s[8:11], 0 offen offset:2048 sc1 lds\n"
"s_waitcnt lgkmcnt(3)\n"
"v_mfma_f32_32x32x16_bf16 v[32:47], a[100:103], v[228:231], v[32:47]\n"
"ds_read_b128 v[244:247], v116 offset:29696\n"
"v_max3_u32 v92, v92, v224, v228\n"
"s_waitcnt lgkmcnt(3)\n"
"v_mfma_f32_32x32x16_bf16 v[32:47], a[104:107], v[232:235], v[32:47]\n"
"ds_read_b128 v[248:251], v116 offset:30720\n"
"s_waitcnt lgkmcnt(3)\n"
"v_mfma_f32_32x32x16_bf16 v[32:47], a[108:111], v[236:239], v[32:47]\n"
"ds_read_b128 v[252:255], v116 offset:31744\n"
"v_max3_u32 v92, v92, v232, v236\n"
"buffer_load_dwordx4 v119, s[8:11], 0 offen offset:3072 sc1 lds\n"
"s_waitcnt lgkmcnt(3)\n"
"v_mfma_f32_32x32x16_bf16 v[32:47], a[112:115], v[240:243], v[32:47]\n"
"s_waitcnt lgkmcnt(2)\n"
"v_mfma_f32_32x32x16_bf16 v[32:47], a[116:119], v[244:247], v[32:47]\n"
"v_max3_u32 v92, v92, v240, v244\n"
"s_waitcnt lgkmcnt(1)\n"
"v_mfma_f32_32x32x16_bf16 v[32:47], a[120:123], v[248:251], v[32:47]\n"
"s_waitcnt lgkmcnt(0)\n"
"v_mfma_f32_32x32x16_bf16 v[32:47], a[124:127], v[252:255], v[32:47]\n"
"v_max3_u32 v92, v92, v248, v252\n"
"v_cmp_eq_u32 vcc, -1, v92\n"
"s_cbranch_vccnz L_fail_o0\n"
"L_ok_o0:\n"
"s_nop 8\n"
"v_cvt_pk_bf16_f32 v104, v32, v33\n"
"v_cvt_pk_bf16_f32 v105, v34, v35\n"
"v_cvt_pk_bf16_f32 v106, v36, v37\n"
"v_cvt_pk_bf16_f32 v107, v38, v39\n"
"v_cvt_pk_bf16_f32 v108, v40, v41\n"
"v_cvt_pk_bf16_f32 v109, v42, v43\n"
"v_cvt_pk_bf16_f32 v110, v44, v45\n"
"v_cvt_pk_bf16_f32 v111, v46, v47\n"
"v_min_u32 v104, -2, v104\n"
"buffer_store_dwordx4 v[104:107], v115, s[8:11], s46 offen sc1\n"
"v_min_u32 v108, -2, v108\n"
"buffer_store_dwordx4 v[108:111], v115, s[8:11], s46 offen offset:1024 sc1\n"
"v_dot2c_f32_bf16 v8, s52, v104\n"
"v_dot2c_f32_bf16 v9, v104, v104\n"
"v_dot2c_f32_bf16 v8, s52, v105\n"
"v_dot2c_f32_bf16 v9, v105, v105\n"
"s_add_u32 m0, s48, 69632\n"
"v_add_u32 v119, 69632, v126\n"
"buffer_load_dwordx4 v119, s[8:11], 0 offen offset:0 sc1 lds\n"
"v_dot2c_f32_bf16 v8, s52, v106\n"
"v_dot2c_f32_bf16 v9, v106, v106\n"
"v_dot2c_f32_bf16 v8, s52, v107\n"
"v_dot2c_f32_bf16 v9, v107, v107\n"
"v_dot2c_f32_bf16 v8, s52, v108\n"
"v_dot2c_f32_bf16 v9, v108, v108\n"
"buffer_load_dwordx4 v119, s[8:11], 0 offen offset:1024 sc1 lds\n"
"v_dot2c_f32_bf16 v8, s52, v109\n"
"v_dot2c_f32_bf16 v9, v109, v109\n"
"v_dot2c_f32_bf16 v8, s52, v110\n"
"v_dot2c_f32_bf16 v9, v110, v110\n"
"v_dot2c_f32_bf16 v8, s52, v111\n"
"v_dot2c_f32_bf16 v9, v111, v111\n"
"v_mov_b32 v40, v0\n"
"v_mov_b32 v41, v1\n"
"v_mov_b32 v42, v2\n"
"v_permlane32_swap_b32 v8, v9\n"
"v_add_f32 v8, v8, v9\n"
"ds_write_b32 v6, v8\n"
"v_mov_b32 v43, v3\n"
"v_mov_b32 v38, v115\n"
"v_mov_b32 v92, v115\n"
"v_mov_b32 v39, v4\n"
"v_mov_b32 v44, -1\n"
"v_mov_b32 v45, -1\n"
"v_mov_b32 v46, -1\n"
"v_mov_b32 v47, -1\n"
"s_add_u32 s45, s45, 2097152\n"
"s_add_u32 s46, s46, 2097152\n"
"s_waitcnt vmcnt(8)\n"
"s_waitcnt lgkmcnt(0)\n"
"s_barrier\n"
"ds_read_b32 v10, v5 offset:0\n"
"ds_read_b32 v11, v5 offset:256\n"
"ds_read_b32 v12, v5 offset:512\n"
"ds_read_b32 v13, v5 offset:768\n"
"buffer_load_dwordx4 v119, s[8:11], 0 offen offset:2048 sc1 lds\n"
"s_waitcnt lgkmcnt(2)\n"
"v_add_f32 v10, v10, v11\n"
"s_waitcnt lgkmcnt(0)\n"
"v_add_f32 v12, v12, v13\n"
"v_add_f32 v10, v10, v12\n"
"v_min_u32 v10, -2, v10\n"
"buffer_store_dword v10, v4, s[8:11], s47 offen sc1\n"
"s_add_u32 s47, s47, 65536\n"
"v_add_u32 v64, s48, v112\n"
"v_add_u32 v65, s48, v113\n"
"v_mov_b32 v66, 0\n"
"v_mov_b32 v67, 0\n"
"v_mov_b32 v68, 0\n"
"v_mov_b32 v69, 0\n"
"v_mov_b32 v70, 0\n"
"v_mov_b32 v71, 0\n"
"ds_read_b128 v[0:3], v64 offset:0\n"
"ds_read_b128 v[4:7], v64 offset:1024\n"
"buffer_load_dwordx4 v119, s[8:11], 0 offen offset:3072 sc1 lds\n"
"ds_read_b128 v[8:11], v64 offset:2048\n"
"ds_read_b128 v[12:15], v64 offset:3072\n"
"ds_read_b128 v[16:19], v64 offset:4096\n"
"ds_read_b128 v[20:23], v64 offset:5120\n"
"ds_read_b128 v[24:27], v64 offset:6144\n"
"ds_read_b128 v[28:31], v64 offset:7168\n"
"ds_read_b128 v[48:51], v64 offset:32768\n"
"s_waitcnt lgkmcnt(8)\n"
"v_dot2c_f32_bf16 v69, s52, v0\n"
"v_dot2c_f32_bf16 v66, s52, v1\n"
"v_dot2c_f32_bf16 v69, s52, v2\n"
"v_dot2c_f32_bf16 v66, s52, v3\n"
"ds_read_b128 v[52:55], v64 offset:33792\n"
"s_waitcnt lgkmcnt(8)\n"
"v_dot2c_f32_bf16 v69, s52, v4\n"
"v_dot2c_f32_bf16 v66, s52, v5\n"
"v_dot2c_f32_bf16 v69, s52, v6\n"
"v_dot2c_f32_bf16 v66, s52, v7\n"
"ds_read_b128 v[56:59], v64 offset:34816\n"
"s_waitcnt lgkmcnt(8)\n"
"v_dot2c_f32_bf16 v69, s52, v8\n"
"v_dot2c_f32_bf16 v66, s52, v9\n"
"v_dot2c_f32_bf16 v69, s52, v10\n"
"v_dot2c_f32_bf16 v66, s52, v11\n"
"s_cmp_lg_u32 s32, 0\n"
"s_cbranch_scc1 L_hi14\n"
"v_lshrrev_b32 v217, 1, v115\n"
"v_accvgpr_read_b32 v213, a129\n"
"v_accvgpr_read_b32 v215, a131\n"
"v_accvgpr_read_b32 v218, a204\n"
"s_mov_b64 exec, s[76:77]\n"
"buffer_load_dwordx2 v[210:211], v215, s[8:11], 0 offen sc1\n"
"s_mov_b64 exec, -1\n"
"L_hi14:\n"
"ds_read_b128 v[60:63], v64 offset:35840\n"
"s_waitcnt lgkmcnt(8)\n"
"v_dot2c_f32_bf16 v69, s52, v12\n"
"v_dot2c_f32_bf16 v66, s52, v13\n"
"v_dot2c_f32_bf16 v69, s52, v14\n"
"v_dot2c_f32_bf16 v66, s52, v15\n"
"ds_read_b128 v[72:75], v64 offset:36864\n"
"s_waitcnt lgkmcnt(8)\n"
"v_dot2c_f32_bf16 v69, s52, v16\n"
"v_dot2c_f32_bf16 v66, s52, v17\n"
"v_dot2c_f32_bf16 v69, s52, v18\n"
"v_dot2c_f32_bf16 v66, s52, v19\n"
"ds_read_b128 v[76:79], v64 offset:37888\n"
"s_waitcnt lgkmcnt(8)\n"
"v_dot2c_f32_bf16 v69, s52, v20\n"
"v_dot2c_f32_bf16 v66, s52, v21\n"
"v_dot2c_f32_bf16 v69, s52, v22\n"
"v_dot2c_f32_bf16 v66, s52, v23\n"
"ds_read_b128 v[80:83], v64 offset:38912\n"
"s_waitcnt lgkmcnt(8)\n"
"v_dot2c_f32_bf16 v69, s52, v24\n"
"v_dot2c_f32_bf16 v66, s52, v25\n"
"v_dot2c_f32_bf16 v69, s52, v26\n"
"v_dot2c_f32_bf16 v66, s52, v27\n"
"ds_read_b128 v[84:87], v64 offset:39936\n"
"s_waitcnt lgkmcnt(8)\n"
"v_dot2c_f32_bf16 v69, s52, v28\n"
"v_dot2c_f32_bf16 v66, s52, v29\n"
"v_dot2c_f32_bf16 v69, s52, v30\n"
"v_dot2c_f32_bf16 v66, s52, v31\n"
"s_waitcnt lgkmcnt(7)\n"
"v_dot2c_f32_bf16 v70, s52, v48\n"
"v_dot2c_f32_bf16 v67, s52, v49\n"
"v_dot2c_f32_bf16 v70, s52, v50\n"
"v_dot2c_f32_bf16 v67, s52, v51\n"
"s_waitcnt lgkmcnt(6)\n"
"v_dot2c_f32_bf16 v70, s52, v52\n"
"v_dot2c_f32_bf16 v67, s52, v53\n"
"v_dot2c_f32_bf16 v70, s52, v54\n"
"v_dot2c_f32_bf16 v67, s52, v55\n"
"s_waitcnt lgkmcnt(5)\n"
"v_dot2c_f32_bf16 v70, s52, v56\n"
"v_dot2c_f32_bf16 v67, s52, v57\n"
"v_dot2c_f32_bf16 v70, s52, v58\n"
"v_dot2c_f32_bf16 v67, s52, v59\n"
"s_waitcnt lgkmcnt(4)\n"
"v_dot2c_f32_bf16 v70, s52, v60\n"
"v_dot2c_f32_bf16 v67, s52, v61\n"
"v_dot2c_f32_bf16 v70, s52, v62\n"
"v_dot2c_f32_bf16 v67, s52, v63\n"
"s_waitcnt lgkmcnt(3)\n"
"v_dot2c_f32_bf16 v70, s52, v72\n"
"v_dot2c_f32_bf16 v67, s52, v73\n"
"v_dot2c_f32_bf16 v70, s52, v74\n"
"v_dot2c_f32_bf16 v67, s52, v75\n"
"s_waitcnt lgkmcnt(2)\n"
"v_dot2c_f32_bf16 v70, s52, v76\n"
"v_dot2c_f32_bf16 v67, s52, v77\n"
"v_dot2c_f32_bf16 v70, s52, v78\n"
"v_dot2c_f32_bf16 v67, s52, v79\n"
"s_waitcnt lgkmcnt(1)\n"
"v_dot2c_f32_bf16 v70, s52, v80\n"
"v_dot2c_f32_bf16 v67, s52, v81\n"
"v_dot2c_f32_bf16 v70, s52, v82\n"
"v_dot2c_f32_bf16 v67, s52, v83\n"
"s_waitcnt lgkmcnt(0)\n"
"v_dot2c_f32_bf16 v70, s52, v84\n"
"v_dot2c_f32_bf16 v67, s52, v85\n"
"v_dot2c_f32_bf16 v70, s52, v86\n"
"v_dot2c_f32_bf16 v67, s52, v87\n"
"v_add_f32 v66, v66, v69\n"
"s_nop 1\n"
"v_add_f32 v67, v67, v70\n"
"v_mov_b32 v0, v66\n"
"v_mov_b32 v1, v67\n"
"v_bfe_u32 v3, v115, 4, 5\n"
"v_permlane32_swap_b32 v66, v0\n"
"v_add_f32 v66, v66, v0\n"
"v_permlane32_swap_b32 v67, v1\n"
"v_add_f32 v67, v67, v1\n"
"s_lshl_b32 s34, s32, 2\n"
"s_add_u32 s34, s34, 159744\n"
"v_lshl_add_u32 v4, v3, 4, s34\n"
"ds_write_b32 v4, v66 offset:0\n"
"ds_write_b32 v4, v67 offset:512\n"
"v_xor_b32 v126, 0x200000, v126\n"
"v_lshrrev_b32 v58, 8, v115\n"
"v_bfe_u32 v119, v115, 4, 4\n"
"v_lshlrev_b32 v59, 2, v58\n"
"v_xor_b32 v119, v119, v59\n"
"v_lshlrev_b32 v119, 4, v119\n"
"v_lshl_add_u32 v119, v58, 10, v119\n"
"s_lshl_b32 s34, s57, 8\n"
"s_lshl_b32 s35, s32, 15\n"
"s_add_u32 s34, s34, s35\n"
"s_add_u32 s34, s34, 0\n"
"v_add_u32 v119, s34, v119\n"
"s_waitcnt vmcnt(5)\n"
"buffer_load_dwordx4 v[128:131], v115, s[8:11], s40 offen offset:0 sc1\n"
"buffer_load_dwordx4 v[132:135], v115, s[8:11], s40 offen offset:1024 sc1\n"
"buffer_load_dwordx4 v[136:139], v115, s[8:11], s40 offen offset:2048 sc1\n"
"buffer_load_dwordx4 v[140:143], v115, s[8:11], s40 offen offset:3072 sc1\n"
"s_add_u32 s78, s40, 4096\n"
"buffer_load_dwordx4 v[144:147], v115, s[8:11], s78 offen offset:0 sc1\n"
"buffer_load_dwordx4 v[148:151], v115, s[8:11], s78 offen offset:1024 sc1\n"
"s_waitcnt vmcnt(6)\n"
"s_barrier\n"
"buffer_load_dwordx4 v[152:155], v115, s[8:11], s78 offen offset:2048 sc1\n"
"ds_read_b128 v[0:3], v65 offset:0\n"
"ds_read_b128 v[4:7], v65 offset:1024\n"
"ds_read_b128 v[8:11], v65 offset:2048\n"
"ds_read_b128 v[12:15], v65 offset:3072\n"
"buffer_load_dwordx4 v[156:159], v115, s[8:11], s78 offen offset:3072 sc1\n"
"ds_read_b128 v[16:19], v65 offset:4096\n"
"ds_read_b128 v[20:23], v65 offset:5120\n"
"ds_read_b128 v[24:27], v65 offset:6144\n"
"ds_read_b128 v[28:31], v65 offset:7168\n"
"s_add_u32 s78, s40, 8192\n"
"buffer_load_dwordx4 v[160:163], v115, s[8:11], s78 offen offset:0 sc1\n"
"s_waitcnt lgkmcnt(7)\n"
"v_dot2c_f32_bf16 v71, s52, v0\n"
"v_dot2c_f32_bf16 v68, s52, v1\n"
"v_dot2c_f32_bf16 v71, s52, v2\n"
"v_dot2c_f32_bf16 v68, s52, v3\n"
"s_waitcnt lgkmcnt(6)\n"
"v_dot2c_f32_bf16 v71, s52, v4\n"
"v_dot2c_f32_bf16 v68, s52, v5\n"
"v_dot2c_f32_bf16 v71, s52, v6\n"
"v_dot2c_f32_bf16 v68, s52, v7\n"
"buffer_load_dwordx4 v[164:167], v115, s[8:11], s78 offen offset:1024 sc1\n"
"s_waitcnt lgkmcnt(5)\n"
"v_dot2c_f32_bf16 v71, s52, v8\n"
"v_dot2c_f32_bf16 v68, s52, v9\n"
"v_dot2c_f32_bf16 v71, s52, v10\n"
"v_dot2c_f32_bf16 v68, s52, v11\n"
"s_waitcnt lgkmcnt(4)\n"
"v_dot2c_f32_bf16 v71, s52, v12\n"
"v_dot2c_f32_bf16 v68, s52, v13\n"
"v_dot2c_f32_bf16 v71, s52, v14\n"
"v_dot2c_f32_bf16 v68, s52, v15\n"
"buffer_load_dwordx4 v[168:171], v115, s[8:11], s78 offen offset:2048 sc1\n"
"s_waitcnt lgkmcnt(3)\n"
"v_dot2c_f32_bf16 v71, s52, v16\n"
"v_dot2c_f32_bf16 v68, s52, v17\n"
"v_dot2c_f32_bf16 v71, s52, v18\n"
"v_dot2c_f32_bf16 v68, s52, v19\n"
"s_waitcnt lgkmcnt(2)\n"
"v_dot2c_f32_bf16 v71, s52, v20\n"
"v_dot2c_f32_bf16 v68, s52, v21\n"
"v_dot2c_f32_bf16 v71, s52, v22\n"
"v_dot2c_f32_bf16 v68, s52, v23\n"
"buffer_load_dwordx4 v[172:175], v115, s[8:11], s78 offen offset:3072 sc1\n"
"s_waitcnt lgkmcnt(1)\n"
"v_dot2c_f32_bf16 v71, s52, v24\n"
"v_dot2c_f32_bf16 v68, s52, v25\n"
"v_dot2c_f32_bf16 v71, s52, v26\n"
"v_dot2c_f32_bf16 v68, s52, v27\n"
"s_waitcnt lgkmcnt(0)\n"
"v_dot2c_f32_bf16 v71, s52, v28\n"
"v_dot2c_f32_bf16 v68, s52, v29\n"
"v_dot2c_f32_bf16 v71, s52, v30\n"
"v_dot2c_f32_bf16 v68, s52, v31\n"
"s_nop 2\n"
"v_add_f32 v68, v68, v71\n"
"v_mov_b32 v0, v68\n"
"v_bfe_u32 v3, v115, 4, 5\n"
"s_nop 0\n"
"v_permlane32_swap_b32 v68, v0\n"
"v_add_f32 v68, v68, v0\n"
"s_lshl_b32 s34, s32, 2\n"
"s_add_u32 s34, s34, 159744\n"
"v_lshl_add_u32 v4, v3, 4, s34\n"
"ds_write_b32 v4, v68 offset:1024\n"
"s_waitcnt lgkmcnt(0)\n"
"s_barrier\n"
"v_bfe_u32 v3, v115, 4, 5\n"
"s_min_u32 s34, s32, 2\n"
"s_lshl_b32 s34, s34, 9\n"
"s_add_u32 s34, s34, 159744\n"
"v_lshl_add_u32 v4, v3, 4, s34\n"
"ds_read_b128 v[6:9], v4\n"
"s_mov_b32 s34, s32\n"
"s_lshl_b32 s34, s34, 9\n"
"s_add_u32 s34, s34, 149504\n"
"v_lshl_add_u32 v4, v3, 4, s34\n"
"s_cmp_lg_u32 s32, 0\n"
"s_cbranch_scc1 L_hc15_done\n"
"s_mov_b64 exec, s[76:77]\n"
"v_cmp_ne_u32 s[34:35], v213, v211\n"
"v_cmp_ne_u32 vcc, v210, v218\n"
"s_and_b64 exec, vcc, s[34:35]\n"
"s_cbranch_execz L_hc15a_noack\n"
"v_mov_b32 v211, v213\n"
"v_mov_b32 v218, v210\n"
"v_accvgpr_write_b32 a204, v210\n"
"buffer_store_dwordx2 v[210:211], v217, s[8:11], s36 offen offset:512 sc1\n"
"L_hc15a_noack:\n"
"s_mov_b64 exec, -1\n"
"s_cmp_eq_u64 s[34:35], 0\n"
"s_cbranch_scc0 L_hc15_loop\n"
"L_hc15_done:\n"
"s_waitcnt lgkmcnt(0)\n"
"v_add_f32 v6, v6, v7\n"
"v_add_f32 v8, v8, v9\n"
"v_add_f32 v6, v6, v8\n"
"v_cvt_pk_bf16_f32 v7, v6, v6\n"
"v_lshlrev_b32 v7, 16, v7\n"
"v_sub_f32 v8, v6, v7\n"
"v_cvt_pk_bf16_f32 v6, v7, v8\n"
"v_and_b32 v9, 0xffff0000, v6\n"
"v_sub_f32 v8, v8, v9\n"
"v_mov_b32 v9, 0\n"
"v_cvt_pk_bf16_f32 v7, v8, v9\n"
"v_mov_b32 v8, 0\n"
"ds_write_b128 v4, v[6:9]\n"
"s_branch L_q1\n"
"L_q:\n"
"buffer_load_dwordx4 v[128:131], v115, s[8:11], s40 offen offset:0 sc1\n"
"buffer_load_dwordx4 v[132:135], v115, s[8:11], s40 offen offset:1024 sc1\n"
"buffer_load_dwordx4 v[136:139], v115, s[8:11], s40 offen offset:2048 sc1\n"
"buffer_load_dwordx4 v[140:143], v115, s[8:11], s40 offen offset:3072 sc1\n"
"s_add_u32 s78, s40, 4096\n"
"buffer_load_dwordx4 v[144:147], v115, s[8:11], s78 offen offset:0 sc1\n"
"buffer_load_dwordx4 v[148:151], v115, s[8:11], s78 offen offset:1024 sc1\n"
"buffer_load_dwordx4 v[152:155], v115, s[8:11], s78 offen offset:2048 sc1\n"
"buffer_load_dwordx4 v[156:159], v115, s[8:11], s78 offen offset:3072 sc1\n"
"s_add_u32 s78, s40, 8192\n"
"buffer_load_dwordx4 v[160:163], v115, s[8:11], s78 offen offset:0 sc1\n"
"buffer_load_dwordx4 v[164:167], v115, s[8:11], s78 offen offset:1024 sc1\n"
"buffer_load_dwordx4 v[168:171], v115, s[8:11], s78 offen offset:2048 sc1\n"
"buffer_load_dwordx4 v[172:175], v115, s[8:11], s78 offen offset:3072 sc1\n"
"s_waitcnt vmcnt(21)\n"
"s_waitcnt lgkmcnt(0)\n"
"s_barrier\n"
"L_q1:\n"
"ds_read_b128 v[48:51], v112 offset:0\n"
"ds_read_b128 v[52:55], v112 offset:32768\n"
"ds_read_b128 v[56:59], v112 offset:1024\n"
"ds_read_b128 v[60:63], v112 offset:33792\n"
"s_waitcnt vmcnt(11)\n"
"v_cmp_eq_u32 vcc, -1, v128\n"
"s_cbranch_vccnz L_xs0\n"
"L_xr0:\n"
"s_waitcnt vmcnt(10) lgkmcnt(2)\n"
"v_mfma_f32_32x32x16_bf16 v[0:15], v[48:51], v[128:131], 0\n"
"v_cmp_eq_u32 vcc, -1, v132\n"
"ds_read_b128 v[64:67], v112 offset:2048\n"
"s_add_u32 s78, s40, 12288\n"
"buffer_load_dwordx4 v[176:179], v115, s[8:11], s78 offen offset:0 sc1\n"
"v_mfma_f32_32x32x16_bf16 v[16:31], v[52:55], v[128:131], 0\n"
"s_cbranch_vccnz L_xs1\n"
"L_xr1:\n"
"ds_read_b128 v[68:71], v112 offset:34816\n"
"s_waitcnt vmcnt(10) lgkmcnt(2)\n"
"v_mfma_f32_32x32x16_bf16 v[0:15], v[56:59], v[132:135], v[0:15]\n"
"v_cmp_eq_u32 vcc, -1, v136\n"
"ds_read_b128 v[48:51], v112 offset:3072\n"
"buffer_load_dwordx4 v[180:183], v115, s[8:11], s78 offen offset:1024 sc1\n"
"v_mfma_f32_32x32x16_bf16 v[16:31], v[60:63], v[132:135], v[16:31]\n"
"s_cbranch_vccnz L_xs2\n"
"L_xr2:\n"
"ds_read_b128 v[52:55], v112 offset:35840\n"
"s_mov_b32 m0, s50\n"
"s_add_u32 s35, s41, 3072\n"
"buffer_load_dwordx4 v40, s[8:11], s41 offen offset:0 sc1 lds\n"
"buffer_load_dwordx4 v40, s[8:11], s35 offen offset:1024 sc1 lds\n"
"s_waitcnt vmcnt(12) lgkmcnt(2)\n"
"v_mfma_f32_32x32x16_bf16 v[0:15], v[64:67], v[136:139], v[0:15]\n"
"v_cmp_eq_u32 vcc, -1, v140\n"
"ds_read_b128 v[56:59], v112 offset:4096\n"
"buffer_load_dwordx4 v[184:187], v115, s[8:11], s78 offen offset:2048 sc1\n"
"v_mfma_f32_32x32x16_bf16 v[16:31], v[68:71], v[136:139], v[16:31]\n"
"s_cbranch_vccnz L_xs3\n"
"L_xr3:\n"
"ds_read_b128 v[60:63], v112 offset:36864\n"
"s_waitcnt vmcnt(12) lgkmcnt(2)\n"
"v_mfma_f32_32x32x16_bf16 v[0:15], v[48:51], v[140:143], v[0:15]\n"
"v_cmp_eq_u32 vcc, -1, v144\n"
"ds_read_b128 v[64:67], v112 offset:5120\n"
"buffer_load_dwordx4 v[188:191], v115, s[8:11], s78 offen offset:3072 sc1\n"
"v_mfma_f32_32x32x16_bf16 v[16:31], v[52:55], v[140:143], v[16:31]\n"
"s_cbranch_vccnz L_xs4\n"
"L_xr4:\n"
"ds_read_b128 v[68:71], v112 offset:37888\n"
"buffer_load_dwordx4 v[32:35], v41, s[8:11], s42 offen sc1\n"
"s_waitcnt vmcnt(13) lgkmcnt(2)\n"
"v_mfma_f32_32x32x16_bf16 v[0:15], v[56:59], v[144:147], v[0:15]\n"
"v_cmp_eq_u32 vcc, -1, v148\n"
"ds_read_b128 v[48:51], v112 offset:6144\n"
"s_add_u32 s78, s40, 16384\n"
"buffer_load_dwordx4 v[192:195], v115, s[8:11], s78 offen offset:0 sc1\n"
"v_mfma_f32_32x32x16_bf16 v[16:31], v[60:63], v[144:147], v[16:31]\n"
"s_cbranch_vccnz L_xs5\n"
"L_xr5:\n"
"ds_read_b128 v[52:55], v112 offset:38912\n"
"buffer_load_dwordx2 v[36:37], v42, s[8:11], s43 offen sc1\n"
"s_waitcnt vmcnt(14) lgkmcnt(2)\n"
"v_mfma_f32_32x32x16_bf16 v[0:15], v[64:67], v[148:151], v[0:15]\n"
"v_cmp_eq_u32 vcc, -1, v152\n"
"ds_read_b128 v[56:59], v112 offset:7168\n"
"buffer_load_dwordx4 v[196:199], v115, s[8:11], s78 offen offset:1024 sc1\n"
"v_mfma_f32_32x32x16_bf16 v[16:31], v[68:71], v[148:151], v[16:31]\n"
"s_cbranch_vccnz L_xs6\n"
"L_xr6:\n"
"ds_read_b128 v[60:63], v112 offset:39936\n"
"s_waitcnt vmcnt(14) lgkmcnt(2)\n"
"v_mfma_f32_32x32x16_bf16 v[0:15], v[48:51], v[152:155], v[0:15]\n"
"v_cmp_eq_u32 vcc, -1, v156\n"
"ds_read_b128 v[64:67], v112 offset:8192\n"
"buffer_load_dwordx4 v[200:203], v115, s[8:11], s78 offen offset:2048 sc1\n"
"v_mfma_f32_32x32x16_bf16 v[16:31], v[52:55], v[152:155], v[16:31]\n"
"s_cbranch_vccnz L_xs7\n"
"L_xr7:\n"
"ds_read_b128 v[68:71], v112 offset:40960\n"
"s_waitcnt vmcnt(14) lgkmcnt(2)\n"
"v_mfma_f32_32x32x16_bf16 v[0:15], v[56:59], v[156:159], v[0:15]\n"
"v_cmp_eq_u32 vcc, -1, v160\n"
"ds_read_b128 v[48:51], v112 offset:9216\n"
"buffer_load_dwordx4 v[204:207], v115, s[8:11], s78 offen offset:3072 sc1\n"
"v_mfma_f32_32x32x16_bf16 v[16:31], v[60:63], v[156:159], v[16:31]\n"
"s_cbranch_vccnz L_xs8\n"
"L_xr8:\n"
"ds_read_b128 v[52:55], v112 offset:41984\n"
"s_waitcnt vmcnt(14) lgkmcnt(2)\n"
"v_mfma_f32_32x32x16_bf16 v[0:15], v[64:67], v[160:163], v[0:15]\n"
"v_cmp_eq_u32 vcc, -1, v164\n"
"ds_read_b128 v[56:59], v112 offset:10240\n"
"s_add_u32 s78, s40, 20480\n"
"buffer_load_dwordx4 v[208:211], v115, s[8:11], s78 offen offset:0 sc1\n"
"v_mfma_f32_32x32x16_bf16 v[16:31], v[68:71], v[160:163], v[16:31]\n"
"s_cbranch_vccnz L_xs9\n"
"L_xr9:\n"
"ds_read_b128 v[60:63], v112 offset:43008\n"
"s_waitcnt vmcnt(14) lgkmcnt(2)\n"
"v_mfma_f32_32x32x16_bf16 v[0:15], v[48:51], v[164:167], v[0:15]\n"
"v_cmp_eq_u32 vcc, -1, v168\n"
"ds_read_b128 v[64:67], v112 offset:11264\n"
"buffer_load_dwordx4 v[212:215], v115, s[8:11], s78 offen offset:1024 sc1\n"
"v_mfma_f32_32x32x16_bf16 v[16:31], v[52:55], v[164:167], v[16:31]\n"
"s_cbranch_vccnz L_xs10\n"
"L_xr10:\n"
"ds_read_b128 v[68:71], v112 offset:44032\n"
"s_waitcnt vmcnt(14) lgkmcnt(2)\n"
"v_mfma_f32_32x32x16_bf16 v[0:15], v[56:59], v[168:171], v[0:15]\n"
"v_cmp_eq_u32 vcc, -1, v172\n"
"ds_read_b128 v[48:51], v112 offset:12288\n"
"buffer_load_dwordx4 v[216:219], v115, s[8:11], s78 offen offset:2048 sc1\n"
"v_mfma_f32_32x32x16_bf16 v[16:31], v[60:63], v[168:171], v[16:31]\n"
"s_cbranch_vccnz L_xs11\n"
"L_xr11:\n"
"ds_read_b128 v[52:55], v112 offset:45056\n"
"s_waitcnt vmcnt(14) lgkmcnt(2)\n"
"v_mfma_f32_32x32x16_bf16 v[0:15], v[64:67], v[172:175], v[0:15]\n"
"v_cmp_eq_u32 vcc, -1, v176\n"
"ds_read_b128 v[56:59], v112 offset:13312\n"
"buffer_load_dwordx4 v[220:223], v115, s[8:11], s78 offen offset:3072 sc1\n"
"v_mfma_f32_32x32x16_bf16 v[16:31], v[68:71], v[172:175], v[16:31]\n"
"s_cbranch_vccnz L_xs12\n"
"L_xr12:\n"
"ds_read_b128 v[60:63], v112 offset:46080\n"
"s_waitcnt vmcnt(12)\n"
"ds_read_b128 v[80:83], v43 offset:0\n"
"ds_read_b128 v[84:87], v43 offset:1024\n"
"s_waitcnt lgkmcnt(4)\n"
"v_mfma_f32_32x32x16_bf16 v[0:15], v[48:51], v[176:179], v[0:15]\n"
"v_cmp_eq_u32 vcc, -1, v180\n"
"ds_read_b128 v[64:67], v112 offset:14336\n"
"s_add_u32 s78, s40, 24576\n"
"buffer_load_dwordx4 v[224:227], v115, s[8:11], s78 offen offset:0 sc1\n"
"v_mfma_f32_32x32x16_bf16 v[16:31], v[52:55], v[176:179], v[16:31]\n"
"s_cbranch_vccnz L_xs13\n"
"L_xr13:\n"
"ds_read_b128 v[68:71], v112 offset:47104\n"
"s_waitcnt lgkmcnt(2)\n"
"v_max_u32 v80, v80, v84\n"
"v_cmp_eq_u32 vcc, -1, v80\n"
"s_cbranch_vccnz L_hs\n"
"L_hr:\n"
"s_barrier\n"
"ds_read_b128 v[72:75], v118 offset:0\n"
"ds_read_b128 v[76:79], v114 offset:0\n"
"s_waitcnt vmcnt(12)\n"
"v_mfma_f32_32x32x16_bf16 v[0:15], v[56:59], v[180:183], v[0:15]\n"
"v_cmp_eq_u32 vcc, -1, v184\n"
"ds_read_b128 v[48:51], v112 offset:15360\n"
"buffer_load_dwordx4 v[228:231], v115, s[8:11], s78 offen offset:1024 sc1\n"
"v_mfma_f32_32x32x16_bf16 v[16:31], v[60:63], v[180:183], v[16:31]\n"
"s_cbranch_vccnz L_xs14\n"
"L_xr14:\n"
"ds_read_b128 v[52:55], v112 offset:48128\n"
"ds_read_b128 v[80:83], v118 offset:512\n"
"ds_read_b128 v[84:87], v114 offset:2048\n"
"s_waitcnt vmcnt(12) lgkmcnt(4)\n"
"v_mfma_f32_32x32x16_bf16 v[0:15], v[64:67], v[184:187], v[0:15]\n"
"v_cmp_eq_u32 vcc, -1, v188\n"
"ds_read_b128 v[56:59], v112 offset:16384\n"
"buffer_load_dwordx4 v[232:235], v115, s[8:11], s78 offen offset:2048 sc1\n"
"v_mfma_f32_16x16x32_bf16 v[88:91], v[76:79], v[72:75], 0\n"
"ds_read_b128 v[72:75], v118 offset:1024\n"
"ds_read_b128 v[76:79], v114 offset:4096\n"
"v_mfma_f32_32x32x16_bf16 v[16:31], v[68:71], v[184:187], v[16:31]\n"
"s_cbranch_vccnz L_xs15\n"
"L_xr15:\n"
"ds_read_b128 v[60:63], v112 offset:49152\n"
"s_waitcnt vmcnt(12)\n"
"v_max3_u32 v93, v32, v33, v34\n"
"s_waitcnt vmcnt(10)\n"
"v_max3_u32 v93, v93, v35, v36\n"
"v_max_u32 v93, v93, v37\n"
"v_cmp_eq_u32 vcc, -1, v93\n"
"s_cbranch_vccnz L_ss\n"
"L_sr:\n"
"s_waitcnt lgkmcnt(4)\n"
"v_mfma_f32_32x32x16_bf16 v[0:15], v[48:51], v[188:191], v[0:15]\n"
"v_cmp_eq_u32 vcc, -1, v192\n"
"ds_read_b128 v[64:67], v112 offset:17408\n"
"buffer_load_dwordx4 v[236:239], v115, s[8:11], s78 offen offset:3072 sc1\n"
"v_add_f32 v32, v32, v34\n"
"v_add_f32 v33, v33, v35\n"
"v_mfma_f32_16x16x32_bf16 v[88:91], v[84:87], v[80:83], v[88:91]\n"
"ds_read_b128 v[80:83], v118 offset:1536\n"
"ds_read_b128 v[84:87], v114 offset:6144\n"
"v_mfma_f32_32x32x16_bf16 v[16:31], v[52:55], v[188:191], v[16:31]\n"
"s_cbranch_vccnz L_xs16\n"
"L_xr16:\n"
"ds_read_b128 v[68:71], v112 offset:50176\n"
"v_permlane32_swap_b32 v32, v33\n"
"s_waitcnt vmcnt(10) lgkmcnt(4)\n"
"v_mfma_f32_32x32x16_bf16 v[0:15], v[56:59], v[192:195], v[0:15]\n"
"v_cmp_eq_u32 vcc, -1, v196\n"
"ds_read_b128 v[48:51], v112 offset:18432\n"
"s_add_u32 s78, s40, 28672\n"
"buffer_load_dwordx4 v[240:243], v115, s[8:11], s78 offen offset:0 sc1\n"
"v_mfma_f32_16x16x32_bf16 v[88:91], v[76:79], v[72:75], v[88:91]\n"
"ds_read_b128 v[72:75], v118 offset:2048\n"
"ds_read_b128 v[76:79], v114 offset:8192\n"
"v_mfma_f32_32x32x16_bf16 v[16:31], v[60:63], v[192:195], v[16:31]\n"
"s_cbranch_vccnz L_xs17\n"
"L_xr17:\n"
"ds_read_b128 v[52:55], v112 offset:51200\n"
"v_permlane32_swap_b32 v36, v37\n"
"s_waitcnt vmcnt(10) lgkmcnt(4)\n"
"v_mfma_f32_32x32x16_bf16 v[0:15], v[64:67], v[196:199], v[0:15]\n"
"v_cmp_eq_u32 vcc, -1, v200\n"
"ds_read_b128 v[56:59], v112 offset:19456\n"
"buffer_load_dwordx4 v[244:247], v115, s[8:11], s78 offen offset:1024 sc1\n"
"v_mfma_f32_16x16x32_bf16 v[88:91], v[84:87], v[80:83], v[88:91]\n"
"ds_read_b128 v[80:83], v118 offset:2560\n"
"ds_read_b128 v[84:87], v114 offset:10240\n"
"v_mfma_f32_32x32x16_bf16 v[16:31], v[68:71], v[196:199], v[16:31]\n"
"s_cbranch_vccnz L_xs18\n"
"L_xr18:\n"
"ds_read_b128 v[60:63], v112 offset:52224\n"
"v_mov_b32 v34, 0\n"
"v_add_f32 v36, v36, v37\n"
"s_waitcnt vmcnt(10) lgkmcnt(4)\n"
"v_mfma_f32_32x32x16_bf16 v[0:15], v[48:51], v[200:203], v[0:15]\n"
"v_cmp_eq_u32 vcc, -1, v204\n"
"ds_read_b128 v[64:67], v112 offset:20480\n"
"buffer_load_dwordx4 v[248:251], v115, s[8:11], s78 offen offset:2048 sc1\n"
"v_add_f32 v32, v32, v33\n"
"v_mov_b32 v100, 0\n"
"v_mfma_f32_16x16x32_bf16 v[88:91], v[76:79], v[72:75], v[88:91]\n"
"ds_read_b128 v[72:75], v118 offset:3072\n"
"ds_read_b128 v[76:79], v114 offset:12288\n"
"v_mfma_f32_32x32x16_bf16 v[16:31], v[52:55], v[200:203], v[16:31]\n"
"s_cbranch_vccnz L_xs19\n"
"L_xr19:\n"
"ds_read_b128 v[68:71], v112 offset:53248\n"
"v_permlane16_swap_b32 v36, v34\n"
"s_waitcnt vmcnt(10) lgkmcnt(4)\n"
"v_mfma_f32_32x32x16_bf16 v[0:15], v[56:59], v[204:207], v[0:15]\n"
"v_cmp_eq_u32 vcc, -1, v208\n"
"ds_read_b128 v[48:51], v112 offset:21504\n"
"buffer_load_dwordx4 v[252:255], v115, s[8:11], s78 offen offset:3072 sc1\n"
"v_mfma_f32_16x16x32_bf16 v[88:91], v[84:87], v[80:83], v[88:91]\n"
"ds_read_b128 v[80:83], v118 offset:3584\n"
"ds_read_b128 v[84:87], v114 offset:14336\n"
"v_mfma_f32_32x32x16_bf16 v[16:31], v[60:63], v[204:207], v[16:31]\n"
"s_cbranch_vccnz L_xs20\n"
"L_xr20:\n"
"ds_read_b128 v[52:55], v112 offset:54272\n"
"v_add_f32 v36, v36, v34\n"
"s_nop 1\n"
"v_permlane32_swap_b32 v32, v36\n"
"s_waitcnt vmcnt(10) lgkmcnt(4)\n"
"v_mfma_f32_32x32x16_bf16 v[0:15], v[64:67], v[208:211], v[0:15]\n"
"v_cmp_eq_u32 vcc, -1, v212\n"
"ds_read_b128 v[56:59], v112 offset:22528\n"
"v_mfma_f32_16x16x32_bf16 v[88:91], v[76:79], v[72:75], v[88:91]\n"
"ds_read_b128 v[72:75], v118 offset:4096\n"
"ds_read_b128 v[76:79], v114 offset:16384\n"
"v_mfma_f32_32x32x16_bf16 v[16:31], v[68:71], v[208:211], v[16:31]\n"
"s_cbranch_vccnz L_xs21\n"
"L_xr21:\n"
"ds_read_b128 v[60:63], v112 offset:55296\n"
"v_mul_f32 v32, 0xbb000000, v32\n"
"v_mul_f32 v36, 0x3b000000, v36\n"
"s_waitcnt vmcnt(9) lgkmcnt(4)\n"
"v_mfma_f32_32x32x16_bf16 v[0:15], v[48:51], v[212:215], v[0:15]\n"
"v_cmp_eq_u32 vcc, -1, v216\n"
"ds_read_b128 v[64:67], v112 offset:23552\n"
"buffer_store_dwordx4 v[44:47], v38, s[8:11], s49 offen offset:0 sc1\n"
"v_fma_f32 v36, -v32, v32, v36\n"
"v_cvt_pk_bf16_f32 v96, v32, v32\n"
"v_mfma_f32_16x16x32_bf16 v[88:91], v[84:87], v[80:83], v[88:91]\n"
"ds_read_b128 v[80:83], v118 offset:4608\n"
"ds_read_b128 v[84:87], v114 offset:18432\n"
"v_mfma_f32_32x32x16_bf16 v[16:31], v[52:55], v[212:215], v[16:31]\n"
"s_cbranch_vccnz L_xs22\n"
"L_xr22:\n"
"ds_read_b128 v[68:71], v112 offset:56320\n"
"v_max_f32 v36, 0, v36\n"
"s_waitcnt vmcnt(9) lgkmcnt(4)\n"
"v_mfma_f32_32x32x16_bf16 v[0:15], v[56:59], v[216:219], v[0:15]\n"
"v_cmp_eq_u32 vcc, -1, v220\n"
"ds_read_b128 v[48:51], v112 offset:24576\n"
"buffer_store_dwordx4 v[44:47], v38, s[8:11], s49 offen offset:1024 sc1\n"
"v_cvt_pk_bf16_f32 v36, v36, v36\n"
"v_permlane32_swap_b32 v96, v100\n"
"v_mfma_f32_16x16x32_bf16 v[88:91], v[76:79], v[72:75], v[88:91]\n"
"ds_read_b128 v[72:75], v118 offset:5120\n"
"ds_read_b128 v[76:79], v114 offset:20480\n"
"v_mfma_f32_32x32x16_bf16 v[16:31], v[60:63], v[216:219], v[16:31]\n"
"s_cbranch_vccnz L_xs23\n"
"L_xr23:\n"
"ds_read_b128 v[52:55], v112 offset:57344\n"
"s_waitcnt vmcnt(9) lgkmcnt(4)\n"
"v_mfma_f32_32x32x16_bf16 v[0:15], v[64:67], v[220:223], v[0:15]\n"
"v_cmp_eq_u32 vcc, -1, v224\n"
"ds_read_b128 v[56:59], v112 offset:25600\n"
"buffer_store_dwordx4 v[44:47], v92, s[8:11], s54 offen offset:0 sc1\n"
"v_lshlrev_b32 v36, 16, v36\n"
"v_mfma_f32_16x16x32_bf16 v[88:91], v[84:87], v[80:83], v[88:91]\n"
"ds_read_b128 v[80:83], v118 offset:5632\n"
"ds_read_b128 v[84:87], v114 offset:22528\n"
"v_mfma_f32_32x32x16_bf16 v[16:31], v[68:71], v[220:223], v[16:31]\n"
"s_cbranch_vccnz L_xs24\n"
"L_xr24:\n"
"ds_read_b128 v[60:63], v112 offset:58368\n"
"v_add_f32 v36, 0x3727c5ac, v36\n"
"v_and_b32 v97, 0xffff, v96\n"
"s_waitcnt vmcnt(9) lgkmcnt(4)\n"
"v_mfma_f32_32x32x16_bf16 v[0:15], v[48:51], v[224:227], v[0:15]\n"
"v_cmp_eq_u32 vcc, -1, v228\n"
"ds_read_b128 v[64:67], v112 offset:26624\n"
"buffer_store_dwordx4 v[44:47], v92, s[8:11], s54 offen offset:1024 sc1\n"
"v_cvt_pk_bf16_f32 v36, v36, v36\n"
"v_and_b32 v101, 0xffff, v100\n"
"v_mfma_f32_16x16x32_bf16 v[88:91], v[76:79], v[72:75], v[88:91]\n"
"ds_read_b128 v[72:75], v118 offset:6144\n"
"ds_read_b128 v[76:79], v114 offset:24576\n"
"v_mfma_f32_32x32x16_bf16 v[16:31], v[52:55], v[224:227], v[16:31]\n"
"s_cbranch_vccnz L_xs25\n"
"L_xr25:\n"
"ds_read_b128 v[68:71], v112 offset:59392\n"
"v_lshlrev_b32 v36, 16, v36\n"
"v_sqrt_f32 v36, v36\n"
"s_waitcnt vmcnt(9) lgkmcnt(4)\n"
"v_mfma_f32_32x32x16_bf16 v[0:15], v[56:59], v[228:231], v[0:15]\n"
"v_cmp_eq_u32 vcc, -1, v232\n"
"ds_read_b128 v[48:51], v112 offset:27648\n"
"buffer_store_dword v44, v39, s[8:11], s60 offen sc1\n"
"v_mfma_f32_16x16x32_bf16 v[88:91], v[84:87], v[80:83], v[88:91]\n"
"ds_read_b128 v[80:83], v118 offset:6656\n"
"ds_read_b128 v[84:87], v114 offset:26624\n"
"v_mfma_f32_32x32x16_bf16 v[16:31], v[60:63], v[228:231], v[16:31]\n"
"s_cbranch_vccnz L_xs26\n"
"L_xr26:\n"
"ds_read_b128 v[52:55], v112 offset:60416\n"
"v_cvt_pk_bf16_f32 v36, v36, v36\n"
"v_lshlrev_b32 v36, 16, v36\n"
"s_waitcnt vmcnt(9) lgkmcnt(4)\n"
"v_mfma_f32_32x32x16_bf16 v[0:15], v[64:67], v[232:235], v[0:15]\n"
"v_cmp_eq_u32 vcc, -1, v236\n"
"ds_read_b128 v[56:59], v112 offset:28672\n"
"v_rcp_f32 v93, v36\n"
"s_nop 0\n"
"ds_write_b32 v127, v93\n"
"v_mfma_f32_16x16x32_bf16 v[88:91], v[76:79], v[72:75], v[88:91]\n"
"ds_read_b128 v[72:75], v118 offset:7168\n"
"ds_read_b128 v[76:79], v114 offset:28672\n"
"v_mfma_f32_32x32x16_bf16 v[16:31], v[68:71], v[232:235], v[16:31]\n"
"s_cbranch_vccnz L_xs27\n"
"L_xr27:\n"
"ds_read_b128 v[60:63], v112 offset:61440\n"
"v_mul_f32 v93, 0x3eb8aa3b, v93\n"
"s_waitcnt vmcnt(8) lgkmcnt(5)\n"
"v_mfma_f32_32x32x16_bf16 v[0:15], v[48:51], v[236:239], v[0:15]\n"
"v_cmp_eq_u32 vcc, -1, v240\n"
"ds_read_b128 v[64:67], v112 offset:29696\n"
"v_mov_b32 v35, v93\n"
"s_nop 1\n"
"v_permlane32_swap_b32 v93, v35\n"
"v_mfma_f32_16x16x32_bf16 v[88:91], v[84:87], v[80:83], v[88:91]\n"
"ds_read_b128 v[80:83], v118 offset:7680\n"
"ds_read_b128 v[84:87], v114 offset:30720\n"
"v_mfma_f32_32x32x16_bf16 v[16:31], v[52:55], v[236:239], v[16:31]\n"
"s_cbranch_vccnz L_xs28\n"
"L_xr28:\n"
"ds_read_b128 v[68:71], v112 offset:62464\n"
"s_waitcnt vmcnt(7) lgkmcnt(4)\n"
"v_mfma_f32_32x32x16_bf16 v[0:15], v[56:59], v[240:243], v[0:15]\n"
"v_cmp_eq_u32 vcc, -1, v244\n"
"ds_read_b128 v[48:51], v112 offset:30720\n"
"ds_read_b128 v[40:43], v94 offset:512\n"
"v_and_b32 v34, 0xfffffcff, v94\n"
"v_mfma_f32_16x16x32_bf16 v[88:91], v[76:79], v[72:75], v[88:91]\n"
"ds_read_b128 v[44:47], v94\n"
"v_mfma_f32_32x32x16_bf16 v[16:31], v[60:63], v[240:243], v[16:31]\n"
"s_cbranch_vccnz L_xs29\n"
"L_xr29:\n"
"ds_read_b128 v[52:55], v112 offset:63488\n"
"v_add_u32 v34, s53, v34\n"
"s_waitcnt vmcnt(6) lgkmcnt(4)\n"
"v_mfma_f32_32x32x16_bf16 v[0:15], v[64:67], v[244:247], v[0:15]\n"
"v_cmp_eq_u32 vcc, -1, v248\n"
"ds_read_b128 v[56:59], v112 offset:31744\n"
"ds_read_b128 v[36:39], v34\n"
"v_mfma_f32_16x16x32_bf16 v[88:91], v[84:87], v[80:83], v[88:91]\n"
"ds_read_b128 v[72:75], v116 offset:53248\n"
"v_mfma_f32_32x32x16_bf16 v[16:31], v[68:71], v[244:247], v[16:31]\n"
"s_cbranch_vccnz L_xs30\n"
"L_xr30:\n"
"ds_read_b128 v[60:63], v112 offset:64512\n"
"ds_read_b128 v[76:79], v116 offset:54272\n"
"s_waitcnt vmcnt(5) lgkmcnt(5)\n"
"v_mfma_f32_32x32x16_bf16 v[0:15], v[48:51], v[248:251], v[0:15]\n"
"v_cmp_eq_u32 vcc, -1, v252\n"
"ds_read_b128 v[64:67], v113 offset:0\n"
"ds_read_b128 v[80:83], v116 offset:55296\n"
"v_mfma_f32_32x32x16_bf16 v[16:31], v[52:55], v[248:251], v[16:31]\n"
"s_cbranch_vccnz L_xs31\n"
"L_xr31:\n"
"ds_read_b128 v[68:71], v113 offset:1024\n"
"s_waitcnt lgkmcnt(4)\n"
"v_mfma_f32_32x32x16_bf16 v[0:15], v[56:59], v[252:255], v[0:15]\n"
"ds_read_b128 v[48:51], v113 offset:2048\n"
"ds_read_b128 v[84:87], v116 offset:56320\n"
"v_mfma_f32_32x32x16_bf16 v[16:31], v[60:63], v[252:255], v[16:31]\n"
"s_cmp_eq_u32 s33, 2\n"
"s_cbranch_scc1 L_t2\n"
"s_cmp_eq_u32 s33, 3\n"
"s_cbranch_scc1 L_t3\n"
"L_t1:\n"
"v_mfma_f32_16x16x32_bf16 v[88:91], v[36:39], v[100:103], v[88:91]\n"
"v_mfma_f32_32x32x16_bf16 v[16:31], v[40:43], v[96:99], v[16:31]\n"
"v_mfma_f32_32x32x16_bf16 v[0:15], v[44:47], v[96:99], v[0:15]\n"
"s_waitcnt lgkmcnt(4)\n"
"v_mfma_f32_32x32x16_bf16 v[32:47], v[128:131], v[64:67], 0\n"
"ds_read_b128 v[52:55], v113 offset:3072\n"
"s_nop 2\n"
"v_cvt_pk_bf16_f32 v88, v88, v89\n"
"v_cvt_pk_bf16_f32 v89, v90, v91\n"
"s_cmp_lt_u32 s32, 2\n"
"s_cbranch_scc1 L_hk16\n"
"ds_write_b16 v121, v88\n"
"ds_write_b16_d16_hi v121, v88 offset:16\n"
"ds_write_b16 v121, v89 offset:32\n"
"ds_write_b16_d16_hi v121, v89 offset:48\n"
"s_branch L_hd17\n"
"L_hk16:\n"
"ds_write_b64 v121, v[88:89]\n"
"L_hd17:\n"
"s_waitcnt lgkmcnt(4)\n"
"v_mfma_f32_32x32x16_bf16 v[32:47], v[132:135], v[68:71], v[32:47]\n"
"ds_read_b128 v[56:59], v113 offset:4096\n"
"v_cvt_pk_bf16_f32 v16, v16, v17\n"
"v_cvt_pk_bf16_f32 v17, v18, v19\n"
"v_cvt_pk_bf16_f32 v18, v20, v21\n"
"v_cvt_pk_bf16_f32 v19, v22, v23\n"
"v_cvt_pk_bf16_f32 v20, v24, v25\n"
"v_cvt_pk_bf16_f32 v21, v26, v27\n"
"v_cvt_pk_bf16_f32 v22, v28, v29\n"
"s_waitcnt lgkmcnt(4)\n"
"v_mfma_f32_32x32x16_bf16 v[32:47], v[136:139], v[48:51], v[32:47]\n"
"v_cvt_pk_bf16_f32 v23, v30, v31\n"
"ds_write_b128 v117, v[16:19] offset:35840\n"
"ds_write_b128 v117, v[20:23] offset:36864\n"
"ds_read_b128 v[48:51], v113 offset:5120\n"
"ds_read_b128 v[88:91], v95\n"
"v_cvt_pk_bf16_f32 v24, v0, v1\n"
"v_cvt_pk_bf16_f32 v25, v2, v3\n"
"s_waitcnt lgkmcnt(6)\n"
"v_mfma_f32_32x32x16_bf16 v[32:47], v[140:143], v[52:55], v[32:47]\n"
"v_cvt_pk_bf16_f32 v26, v4, v5\n"
"v_cvt_pk_bf16_f32 v27, v6, v7\n"
"v_cvt_pk_bf16_f32 v28, v8, v9\n"
"v_cvt_pk_bf16_f32 v29, v10, v11\n"
"v_cvt_pk_bf16_f32 v30, v12, v13\n"
"v_cvt_pk_bf16_f32 v31, v14, v15\n"
"v_mfma_f32_32x32x16_bf16 v[0:15], v[16:19], v[24:27], v[72:87]\n"
"s_waitcnt lgkmcnt(0)\n"
"s_barrier\n"
"v_mfma_f32_32x32x16_bf16 v[72:87], v[20:23], v[28:31], v[72:87]\n"
"ds_read_b128 v[64:67], v117 offset:32768\n"
"ds_read_b128 v[68:71], v117 offset:33792\n"
"ds_read_b128 v[52:55], v113 offset:6144\n"
"v_mfma_f32_32x32x16_bf16 v[32:47], v[144:147], v[56:59], v[32:47]\n"
"ds_read_b32 v60, v122\n"
"ds_read_b32 v61, v123\n"
"ds_read_b32 v62, v124\n"
"ds_read_b32 v63, v125\n"
"ds_read_b128 v[56:59], v113 offset:7168\n"
"v_mfma_f32_32x32x16_bf16 v[32:47], v[148:151], v[48:51], v[32:47]\n"
"ds_read_b128 v[48:51], v113 offset:8192\n"
"s_waitcnt lgkmcnt(6)\n"
"v_mfma_f32_32x32x16_bf16 v[32:47], v[152:155], v[52:55], v[32:47]\n"
"ds_read_b128 v[52:55], v113 offset:9216\n"
"v_mfma_f32_32x32x16_bf16 v[128:143], v[64:67], v[24:27], v[76:91]\n"
"s_waitcnt lgkmcnt(2)\n"
"v_mfma_f32_32x32x16_bf16 v[32:47], v[156:159], v[56:59], v[32:47]\n"
"ds_read_b128 v[56:59], v113 offset:10240\n"
"v_mul_f32 v16, v60, v93\n"
"v_mul_f32 v17, v61, v93\n"
"v_mul_f32 v18, v62, v93\n"
"v_mul_f32 v19, v63, v93\n"
"s_add_u32 m0, s48, 0\n"
"s_nop 0\n"
"buffer_load_dwordx4 v119, s[24:27], 0 offen offset:0 lds\n"
"v_mfma_f32_32x32x16_bf16 v[144:159], v[68:71], v[28:31], v[76:91]\n"
"v_max3_f32 v20, v0, v4, v8\n"
"v_max3_f32 v21, v1, v5, v9\n"
"v_max3_f32 v22, v2, v6, v10\n"
"v_max3_f32 v23, v3, v7, v11\n"
"buffer_load_dwordx4 v119, s[24:27], s55 offen offset:1024 lds\n"
"s_waitcnt lgkmcnt(2)\n"
"v_mfma_f32_32x32x16_bf16 v[32:47], v[160:163], v[48:51], v[32:47]\n"
"ds_read_b128 v[48:51], v113 offset:11264\n"
"v_max3_f32 v20, v20, v12, v140\n"
"v_max3_f32 v21, v21, v13, v141\n"
"v_max3_f32 v22, v22, v14, v142\n"
"v_max3_f32 v23, v23, v15, v143\n"
"v_mul_f32 v20, v20, v16\n"
"buffer_load_dwordx4 v119, s[24:27], s56 offen offset:2048 lds\n"
"s_waitcnt lgkmcnt(2)\n"
"v_mfma_f32_32x32x16_bf16 v[32:47], v[164:167], v[52:55], v[32:47]\n"
"ds_read_b128 v[52:55], v113 offset:12288\n"
"v_mul_f32 v21, v21, v17\n"
"v_mul_f32 v22, v22, v18\n"
"v_mul_f32 v23, v23, v19\n"
"v_max3_f32 v68, v20, v21, v22\n"
"buffer_load_dwordx4 v119, s[24:27], s63 offen offset:3072 lds\n"
"s_waitcnt lgkmcnt(2)\n"
"v_mfma_f32_32x32x16_bf16 v[32:47], v[168:171], v[56:59], v[32:47]\n"
"ds_read_b128 v[56:59], v113 offset:13312\n"
"v_max_f32 v68, v68, v23\n"
"v_max3_f32 v64, v72, v76, v80\n"
"v_max3_f32 v65, v73, v77, v81\n"
"v_max3_f32 v66, v74, v78, v82\n"
"s_add_u32 m0, s48, 4096\n"
"v_add_u32 v119, 16384, v119\n"
"buffer_load_dwordx4 v119, s[24:27], 0 offen offset:0 lds\n"
"s_waitcnt lgkmcnt(2)\n"
"v_mfma_f32_32x32x16_bf16 v[32:47], v[172:175], v[48:51], v[32:47]\n"
"ds_read_b128 v[48:51], v113 offset:14336\n"
"ds_read_b128 v[160:163], v116 offset:58368\n"
"ds_read_b128 v[164:167], v116 offset:59392\n"
"ds_read_b128 v[168:171], v116 offset:60416\n"
"v_max3_f32 v67, v75, v79, v83\n"
"v_max3_f32 v64, v64, v84, v156\n"
"v_max3_f32 v65, v65, v85, v157\n"
"v_max3_f32 v66, v66, v86, v158\n"
"buffer_load_dwordx4 v119, s[24:27], s55 offen offset:1024 lds\n"
"s_waitcnt lgkmcnt(5)\n"
"v_mfma_f32_32x32x16_bf16 v[32:47], v[176:179], v[52:55], v[32:47]\n"
"ds_read_b128 v[52:55], v113 offset:15360\n"
"v_max3_f32 v67, v67, v87, v159\n"
"v_mul_f32 v64, v64, v16\n"
"v_mul_f32 v65, v65, v17\n"
"v_mul_f32 v66, v66, v18\n"
"buffer_load_dwordx4 v119, s[24:27], s56 offen offset:2048 lds\n"
"s_waitcnt lgkmcnt(5)\n"
"v_mfma_f32_32x32x16_bf16 v[32:47], v[180:183], v[56:59], v[32:47]\n"
"ds_read_b128 v[56:59], v113 offset:16384\n"
"v_mul_f32 v67, v67, v19\n"
"v_max3_f32 v69, v64, v65, v66\n"
"v_max_f32 v69, v69, v67\n"
"s_nop 1\n"
"v_permlane32_swap_b32 v68, v69\n"
"buffer_load_dwordx4 v119, s[24:27], s63 offen offset:3072 lds\n"
"s_waitcnt lgkmcnt(5)\n"
"v_mfma_f32_32x32x16_bf16 v[32:47], v[184:187], v[48:51], v[32:47]\n"
"ds_read_b128 v[48:51], v113 offset:17408\n"
"v_max_f32 v68, v68, v69\n"
"v_mov_b32 v69, v68\n"
"s_nop 1\n"
"v_permlane32_swap_b32 v68, v69\n"
"v_sub_f32 v20, v20, v68\n"
"s_add_u32 m0, s48, 32768\n"
"v_add_u32 v119, 114688, v119\n"
"buffer_load_dwordx4 v119, s[24:27], 0 offen offset:0 lds\n"
"s_waitcnt lgkmcnt(2)\n"
"v_mfma_f32_32x32x16_bf16 v[32:47], v[188:191], v[52:55], v[32:47]\n"
"ds_read_b128 v[52:55], v113 offset:18432\n"
"v_sub_f32 v21, v21, v68\n"
"v_sub_f32 v22, v22, v68\n"
"v_sub_f32 v23, v23, v68\n"
"v_sub_f32 v64, v64, v69\n"
"buffer_load_dwordx4 v119, s[24:27], s55 offen offset:1024 lds\n"
"s_waitcnt lgkmcnt(2)\n"
"v_mfma_f32_32x32x16_bf16 v[32:47], v[192:195], v[56:59], v[32:47]\n"
"ds_read_b128 v[56:59], v113 offset:19456\n"
"v_sub_f32 v65, v65, v69\n"
"v_sub_f32 v66, v66, v69\n"
"v_sub_f32 v67, v67, v69\n"
"v_exp_f32 v20, v20\n"
"v_exp_f32 v21, v21\n"
"buffer_load_dwordx4 v119, s[24:27], s56 offen offset:2048 lds\n"
"s_waitcnt lgkmcnt(2)\n"
"v_mfma_f32_32x32x16_bf16 v[32:47], v[196:199], v[48:51], v[32:47]\n"
"ds_read_b128 v[48:51], v113 offset:20480\n"
"v_exp_f32 v22, v22\n"
"v_exp_f32 v23, v23\n"
"v_exp_f32 v64, v64\n"
"v_exp_f32 v65, v65\n"
"buffer_load_dwordx4 v119, s[24:27], s63 offen offset:3072 lds\n"
"s_waitcnt lgkmcnt(2)\n"
"v_mfma_f32_32x32x16_bf16 v[32:47], v[200:203], v[52:55], v[32:47]\n"
"ds_read_b128 v[52:55], v113 offset:21504\n"
"ds_read_b128 v[196:199], v94 offset:1024\n"
"v_exp_f32 v66, v66\n"
"v_exp_f32 v67, v67\n"
"v_add_f32 v68, v20, v21\n"
"v_add_f32 v70, v22, v23\n"
"s_add_u32 m0, s48, 36864\n"
"v_add_u32 v119, 16384, v119\n"
"buffer_load_dwordx4 v119, s[24:27], 0 offen offset:0 lds\n"
"s_waitcnt lgkmcnt(3)\n"
"v_mfma_f32_32x32x16_bf16 v[32:47], v[204:207], v[56:59], v[32:47]\n"
"ds_read_b128 v[56:59], v113 offset:22528\n"
"v_add_f32 v69, v64, v65\n"
"v_add_f32 v71, v66, v67\n"
"v_add_f32 v68, v68, v70\n"
"v_add_f32 v69, v69, v71\n"
"buffer_load_dwordx4 v119, s[24:27], s55 offen offset:1024 lds\n"
"s_waitcnt lgkmcnt(3)\n"
"v_mfma_f32_32x32x16_bf16 v[32:47], v[208:211], v[48:51], v[32:47]\n"
"ds_read_b128 v[48:51], v113 offset:23552\n"
"v_mov_b32 v180, 0\n"
"v_mov_b32 v181, 0\n"
"v_mov_b32 v192, 0\n"
"v_mov_b32 v193, 0\n"
"buffer_load_dwordx4 v119, s[24:27], s56 offen offset:2048 lds\n"
"s_waitcnt lgkmcnt(3)\n"
"v_mfma_f32_32x32x16_bf16 v[32:47], v[212:215], v[52:55], v[32:47]\n"
"ds_read_b128 v[52:55], v113 offset:24576\n"
"v_mul_f32 v20, v20, v60\n"
"v_mul_f32 v21, v21, v61\n"
"v_permlane32_swap_b32 v68, v69\n"
"v_add_f32 v68, v68, v69\n"
"buffer_load_dwordx4 v119, s[24:27], s63 offen offset:3072 lds\n"
"s_waitcnt lgkmcnt(2)\n"
"v_mfma_f32_32x32x16_bf16 v[32:47], v[216:219], v[56:59], v[32:47]\n"
"ds_read_b128 v[56:59], v113 offset:25600\n"
"v_rcp_f32 v68, v68\n"
"v_mul_f32 v22, v22, v62\n"
"v_mov_b32 v69, v68\n"
"v_mul_f32 v23, v23, v63\n"
"s_add_u32 m0, s48, 98304\n"
"v_add_u32 v119, 245760, v119\n"
"buffer_load_dwordx4 v119, s[24:27], 0 offen offset:0 lds\n"
"s_waitcnt lgkmcnt(2)\n"
"v_mfma_f32_32x32x16_bf16 v[32:47], v[220:223], v[48:51], v[32:47]\n"
"ds_read_b128 v[48:51], v113 offset:26624\n"
"v_mul_f32 v64, v64, v60\n"
"v_permlane32_swap_b32 v68, v69\n"
"v_mul_f32 v65, v65, v61\n"
"v_mul_f32 v66, v66, v62\n"
"buffer_load_dwordx4 v119, s[24:27], s55 offen offset:1024 lds\n"
"s_waitcnt lgkmcnt(2)\n"
"v_mfma_f32_32x32x16_bf16 v[32:47], v[224:227], v[52:55], v[32:47]\n"
"ds_read_b128 v[52:55], v113 offset:27648\n"
"v_mul_f32 v67, v67, v63\n"
"v_mul_f32 v20, v20, v68\n"
"v_mul_f32 v21, v21, v68\n"
"v_mul_f32 v22, v22, v68\n"
"v_mul_f32 v23, v23, v68\n"
"buffer_load_dwordx4 v119, s[24:27], s56 offen offset:2048 lds\n"
"s_waitcnt lgkmcnt(2)\n"
"v_mfma_f32_32x32x16_bf16 v[32:47], v[228:231], v[56:59], v[32:47]\n"
"ds_read_b128 v[56:59], v113 offset:28672\n"
"v_cvt_pk_bf16_f32 v20, v20, v21\n"
"v_cvt_pk_bf16_f32 v21, v22, v23\n"
"v_and_b32 v172, v20, v162\n"
"v_and_b32 v173, v21, v163\n"
"buffer_load_dwordx4 v119, s[24:27], s63 offen offset:3072 lds\n"
"s_waitcnt lgkmcnt(2)\n"
"v_mfma_f32_32x32x16_bf16 v[32:47], v[232:235], v[48:51], v[32:47]\n"
"ds_read_b128 v[48:51], v113 offset:29696\n"
"v_and_b32 v174, v20, v164\n"
"v_and_b32 v175, v21, v165\n"
"v_and_b32 v176, v20, v166\n"
"v_and_b32 v177, v21, v167\n"
"s_add_u32 m0, s48, 102400\n"
"v_add_u32 v119, 16384, v119\n"
"buffer_load_dwordx4 v119, s[24:27], 0 offen offset:0 lds\n"
"s_waitcnt lgkmcnt(2)\n"
"v_mfma_f32_32x32x16_bf16 v[32:47], v[236:239], v[52:55], v[32:47]\n"
"ds_read_b128 v[52:55], v113 offset:30720\n"
"v_and_b32 v178, v20, v168\n"
"v_and_b32 v179, v21, v169\n"
"v_and_b32 v182, v20, v160\n"
"v_and_b32 v183, v21, v161\n"
"buffer_load_dwordx4 v119, s[24:27], s55 offen offset:1024 lds\n"
"s_waitcnt lgkmcnt(2)\n"
"v_mfma_f32_32x32x16_bf16 v[32:47], v[240:243], v[56:59], v[32:47]\n"
"ds_read_b128 v[56:59], v113 offset:31744\n"
"v_mul_f32 v64, v64, v69\n"
"v_mul_f32 v65, v65, v69\n"
"v_mul_f32 v66, v66, v69\n"
"v_mul_f32 v67, v67, v69\n"
"buffer_load_dwordx4 v119, s[24:27], s56 offen offset:2048 lds\n"
"s_waitcnt lgkmcnt(2)\n"
"v_mfma_f32_32x32x16_bf16 v[32:47], v[244:247], v[48:51], v[32:47]\n"
"v_cvt_pk_bf16_f32 v64, v64, v65\n"
"v_cvt_pk_bf16_f32 v65, v66, v67\n"
"v_and_b32 v184, v64, v162\n"
"v_and_b32 v185, v65, v163\n"
"buffer_load_dwordx4 v119, s[24:27], s63 offen offset:3072 lds\n"
"s_waitcnt lgkmcnt(1)\n"
"v_mfma_f32_32x32x16_bf16 v[32:47], v[248:251], v[52:55], v[32:47]\n"
"v_and_b32 v186, v64, v164\n"
"v_and_b32 v187, v65, v165\n"
"v_and_b32 v188, v64, v166\n"
"v_and_b32 v189, v65, v167\n"
"s_waitcnt lgkmcnt(0)\n"
"v_mfma_f32_32x32x16_bf16 v[32:47], v[252:255], v[56:59], v[32:47]\n"
"v_and_b32 v190, v64, v168\n"
"v_and_b32 v191, v65, v169\n"
"v_and_b32 v194, v64, v160\n"
"v_and_b32 v195, v65, v161\n"
"v_mfma_f32_32x32x16_bf16 v[32:47], v[96:99], v[196:199], v[32:47]\n"
"s_nop 11\n"
"v_cvt_pk_bf16_f32 v40, v40, v41\n"
"v_cvt_pk_bf16_f32 v41, v42, v43\n"
"v_cvt_pk_bf16_f32 v42, v44, v45\n"
"v_cvt_pk_bf16_f32 v43, v46, v47\n"
"ds_write_b128 v117, v[40:43] offset:37888\n"
"v_cvt_pk_bf16_f32 v32, v32, v33\n"
"v_mfma_f32_32x32x16_bf16 v[0:15], v[40:43], v[176:179], 0\n"
"v_cvt_pk_bf16_f32 v33, v34, v35\n"
"v_cvt_pk_bf16_f32 v34, v36, v37\n"
"v_cvt_pk_bf16_f32 v35, v38, v39\n"
"v_mfma_f32_32x32x16_bf16 v[16:31], v[40:43], v[188:191], 0\n"
"s_waitcnt vmcnt(8)\n"
"s_waitcnt lgkmcnt(0)\n"
"s_barrier\n"
"ds_read_b128 v[44:47], v117 offset:34816\n"
"v_mfma_f32_32x32x16_bf16 v[0:15], v[32:35], v[172:175], v[0:15]\n"
"v_mfma_f32_32x32x16_bf16 v[16:31], v[32:35], v[184:187], v[16:31]\n"
"s_waitcnt lgkmcnt(0)\n"
"v_mfma_f32_32x32x16_bf16 v[0:15], v[44:47], v[180:183], v[0:15]\n"
"v_mfma_f32_32x32x16_bf16 v[16:31], v[44:47], v[192:195], v[16:31]\n"
"s_nop 10\n"
"v_cvt_pk_bf16_f32 v48, v0, v1\n"
"v_cvt_pk_bf16_f32 v49, v2, v3\n"
"v_cvt_pk_bf16_f32 v50, v4, v5\n"
"v_cvt_pk_bf16_f32 v51, v6, v7\n"
"v_min_u32 v48, -2, v48\n"
"buffer_store_dwordx4 v[48:51], v115, s[8:11], s44 offen sc1\n"
"v_cvt_pk_bf16_f32 v52, v24, v25\n"
"v_cvt_pk_bf16_f32 v53, v26, v27\n"
"v_cvt_pk_bf16_f32 v54, v28, v29\n"
"v_cvt_pk_bf16_f32 v55, v30, v31\n"
"v_min_u32 v52, -2, v52\n"
"buffer_store_dwordx4 v[52:55], v115, s[8:11], s44 offen offset:1024 sc1\n"
"s_add_u32 m0, s48, 65536\n"
"v_add_u32 v119, 4294819840, v119\n"
"buffer_load_dwordx4 v119, s[24:27], 0 offen offset:0 lds\n"
"buffer_load_dwordx4 v119, s[24:27], s55 offen offset:1024 lds\n"
"buffer_load_dwordx4 v119, s[24:27], s56 offen offset:2048 lds\n"
"buffer_load_dwordx4 v119, s[24:27], s63 offen offset:3072 lds\n"
"s_add_u32 m0, s48, 69632\n"
"v_add_u32 v119, 16384, v119\n"
"buffer_load_dwordx4 v119, s[24:27], 0 offen offset:0 lds\n"
"buffer_load_dwordx4 v119, s[24:27], s55 offen offset:1024 lds\n"
"buffer_load_dwordx4 v119, s[24:27], s56 offen offset:2048 lds\n"
"buffer_load_dwordx4 v119, s[24:27], s63 offen offset:3072 lds\n"
"v_lshrrev_b32 v58, 9, v115\n"
"v_bfe_u32 v59, v115, 6, 2\n"
"v_lshl_add_u32 v58, v58, 2, v59\n"
"v_lshlrev_b32 v58, 8, v58\n"
"v_xor_b32 v59, s32, v59\n"
"v_lshl_add_u32 v58, v59, 6, v58\n"
"v_bfe_u32 v59, v115, 8, 1\n"
"v_lshl_add_u32 v58, v59, 5, v58\n"
"v_bfe_u32 v59, v115, 4, 2\n"
"v_lshl_add_u32 v58, v59, 3, v58\n"
"v_add_u32 v59, 0x10000, v58\n"
"ds_read_b64_tr_b16 a[128:129], v58 offset:0\n"
"ds_read_b64_tr_b16 a[130:131], v58 offset:2048\n"
"ds_read_b64_tr_b16 a[132:133], v58 offset:4096\n"
"ds_read_b64_tr_b16 a[134:135], v58 offset:6144\n"
"ds_read_b64_tr_b16 a[136:137], v58 offset:8192\n"
"ds_read_b64_tr_b16 a[138:139], v58 offset:10240\n"
"ds_read_b64_tr_b16 a[140:141], v58 offset:12288\n"
"ds_read_b64_tr_b16 a[142:143], v58 offset:14336\n"
"ds_read_b64_tr_b16 a[144:145], v58 offset:16384\n"
"ds_read_b64_tr_b16 a[146:147], v58 offset:18432\n"
"ds_read_b64_tr_b16 a[148:149], v58 offset:20480\n"
"ds_read_b64_tr_b16 a[150:151], v58 offset:22528\n"
"ds_read_b64_tr_b16 a[152:153], v58 offset:24576\n"
"ds_read_b64_tr_b16 a[154:155], v58 offset:26624\n"
"ds_read_b64_tr_b16 a[156:157], v58 offset:28672\n"
"ds_read_b64_tr_b16 a[158:159], v58 offset:30720\n"
"ds_read_b64_tr_b16 a[160:161], v58 offset:32768\n"
"ds_read_b64_tr_b16 a[162:163], v58 offset:34816\n"
"ds_read_b64_tr_b16 a[164:165], v58 offset:36864\n"
"ds_read_b64_tr_b16 a[166:167], v58 offset:38912\n"
"ds_read_b64_tr_b16 a[168:169], v58 offset:40960\n"
"ds_read_b64_tr_b16 a[170:171], v58 offset:43008\n"
"ds_read_b64_tr_b16 a[172:173], v58 offset:45056\n"
"ds_read_b64_tr_b16 a[174:175], v58 offset:47104\n"
"ds_read_b64_tr_b16 a[176:177], v58 offset:49152\n"
"ds_read_b64_tr_b16 a[178:179], v58 offset:51200\n"
"ds_read_b64_tr_b16 a[180:181], v58 offset:53248\n"
"ds_read_b64_tr_b16 a[182:183], v58 offset:55296\n"
"ds_read_b64_tr_b16 a[184:185], v58 offset:57344\n"
"ds_read_b64_tr_b16 a[186:187], v58 offset:59392\n"
"ds_read_b64_tr_b16 a[188:189], v58 offset:61440\n"
"ds_read_b64_tr_b16 a[190:191], v58 offset:63488\n"
"v_lshlrev_b32 v32, 16, v104\n"
"v_and_b32 v33, 0xffff0000, v104\n"
"v_lshlrev_b32 v34, 16, v105\n"
"v_and_b32 v35, 0xffff0000, v105\n"
"v_lshlrev_b32 v36, 16, v106\n"
"v_and_b32 v37, 0xffff0000, v106\n"
"v_lshlrev_b32 v38, 16, v107\n"
"v_and_b32 v39, 0xffff0000, v107\n"
"v_lshlrev_b32 v40, 16, v108\n"
"v_and_b32 v41, 0xffff0000, v108\n"
"v_lshlrev_b32 v42, 16, v109\n"
"v_and_b32 v43, 0xffff0000, v109\n"
"v_lshlrev_b32 v44, 16, v110\n"
"v_and_b32 v45, 0xffff0000, v110\n"
"v_lshlrev_b32 v46, 16, v111\n"
"v_and_b32 v47, 0xffff0000, v111\n"
"v_lshrrev_b32 v56, 2, v115\n"
"v_and_b32 v56, 0x30, v56\n"
"v_lshl_add_u32 v56, v115, 2, v56\n"
"v_add_u32 v56, s51, v56\n"
"s_waitcnt vmcnt(10)\n"
"s_waitcnt lgkmcnt(0)\n"
"s_barrier\n"
"ds_read_b64_tr_b16 a[224:225], v59 offset:32768\n"
"ds_read_b64_tr_b16 a[226:227], v59 offset:34816\n"
"ds_read_b64_tr_b16 a[228:229], v59 offset:36864\n"
"ds_read_b64_tr_b16 a[230:231], v59 offset:38912\n"
"ds_read_b64_tr_b16 a[232:233], v59 offset:40960\n"
"ds_read_b64_tr_b16 a[234:235], v59 offset:43008\n"
"ds_read_b64_tr_b16 a[236:237], v59 offset:45056\n"
"ds_read_b64_tr_b16 a[238:239], v59 offset:47104\n"
"ds_read_b64_tr_b16 a[240:241], v59 offset:49152\n"
"ds_read_b64_tr_b16 a[242:243], v59 offset:51200\n"
"ds_read_b64_tr_b16 a[244:245], v59 offset:53248\n"
"ds_read_b64_tr_b16 a[246:247], v59 offset:55296\n"
"ds_read_b64_tr_b16 a[248:249], v59 offset:57344\n"
"ds_read_b64_tr_b16 a[250:251], v59 offset:59392\n"
"ds_read_b64_tr_b16 a[252:253], v59 offset:61440\n"
"ds_read_b64_tr_b16 a[254:255], v59 offset:63488\n"
"s_waitcnt lgkmcnt(0)\n"
"s_barrier\n"
"s_waitcnt vmcnt(8)\n"
"s_mov_b32 m0, s51\n"
"s_nop 0\n"
"buffer_load_dwordx4 v115, s[8:11], s45 offen offset:0 sc1 lds\n"
"buffer_load_dwordx4 v115, s[8:11], s45 offen offset:1024 sc1 lds\n"
"buffer_load_dwordx4 v115, s[8:11], s45 offen offset:2048 sc1 lds\n"
"buffer_load_dwordx4 v115, s[8:11], s45 offen offset:3072 sc1 lds\n"
"s_add_u32 m0, s51, 16384\n"
"s_add_u32 s35, s45, 16384\n"
"buffer_load_dwordx4 v115, s[8:11], s35 offen offset:0 sc1 lds\n"
"buffer_load_dwordx4 v115, s[8:11], s35 offen offset:1024 sc1 lds\n"
"buffer_load_dwordx4 v115, s[8:11], s35 offen offset:2048 sc1 lds\n"
"buffer_load_dwordx4 v115, s[8:11], s35 offen offset:3072 sc1 lds\n"
"s_add_u32 m0, s48, 0\n"
"v_add_u32 v119, 0, v126\n"
"buffer_load_dwordx4 v119, s[8:11], 0 offen offset:0 sc1 lds\n"
"buffer_load_dwordx4 v119, s[8:11], 0 offen offset:1024 sc1 lds\n"
"buffer_load_dwordx4 v119, s[8:11], 0 offen offset:2048 sc1 lds\n"
"s_waitcnt vmcnt(11)\n"
"s_waitcnt lgkmcnt(0)\n"
"s_barrier\n"
"ds_read_b64_tr_b16 a[192:193], v59 offset:0\n"
"ds_read_b64_tr_b16 a[194:195], v59 offset:2048\n"
"ds_read_b64_tr_b16 a[196:197], v59 offset:4096\n"
"ds_read_b64_tr_b16 a[198:199], v59 offset:6144\n"
"ds_read_b64_tr_b16 a[200:201], v59 offset:8192\n"
"ds_read_b64_tr_b16 a[202:203], v59 offset:10240\n"
"ds_read_b64_tr_b16 a[204:205], v59 offset:12288\n"
"ds_read_b64_tr_b16 a[206:207], v59 offset:14336\n"
"ds_read_b64_tr_b16 a[208:209], v59 offset:16384\n"
"ds_read_b64_tr_b16 a[210:211], v59 offset:18432\n"
"ds_read_b64_tr_b16 a[212:213], v59 offset:20480\n"
"ds_read_b64_tr_b16 a[214:215], v59 offset:22528\n"
"ds_read_b64_tr_b16 a[216:217], v59 offset:24576\n"
"ds_read_b64_tr_b16 a[218:219], v59 offset:26624\n"
"ds_read_b64_tr_b16 a[220:221], v59 offset:28672\n"
"ds_read_b64_tr_b16 a[222:223], v59 offset:30720\n"
"s_add_u32 m0, s48, 0\n"
"v_add_u32 v119, 0, v126\n"
"buffer_load_dwordx4 v119, s[8:11], 0 offen offset:3072 sc1 lds\n"
"s_add_u32 m0, s48, 4096\n"
"v_add_u32 v119, 4096, v126\n"
"buffer_load_dwordx4 v119, s[8:11], 0 offen offset:0 sc1 lds\n"
"buffer_load_dwordx4 v119, s[8:11], 0 offen offset:1024 sc1 lds\n"
"v_lshrrev_b32 v12, 9, v115\n"
"v_bfe_u32 v13, v115, 8, 1\n"
"v_lshl_add_u32 v12, v12, 1, v13\n"
"v_bfe_u32 v13, v115, 7, 1\n"
"v_and_b32 v14, 0x70, v115\n"
"v_lshl_add_u32 v14, v13, 9, v14\n"
"v_lshl_add_u32 v12, v12, 10, v14\n"
"s_cmp_gt_u32 s59, 0\n"
"s_cselect_b32 s34, 384, 1073741824\n"
"s_cselect_b32 s35, 8, 99\n"
"v_add_u32 v0, s34, v12\n"
"v_bfe_u32 v12, v115, 4, 5\n"
"v_lshrrev_b32 v13, 9, v115\n"
"v_lshlrev_b32 v12, 5, v12\n"
"v_lshl_add_u32 v1, v13, 4, v12\n"
"v_bfe_u32 v12, v115, 4, 4\n"
"v_lshrrev_b32 v13, 8, v115\n"
"v_cmp_lt_u32 vcc, s35, v12\n"
"v_lshlrev_b32 v12, 5, v12\n"
"v_lshl_add_u32 v12, v13, 3, v12\n"
"v_add_u32 v12, 512, v12\n"
"v_mov_b32 v13, 1073741824\n"
"v_cndmask_b32 v2, v13, v12\n"
"v_add_u32 v3, s50, v115\n"
"v_bfe_u32 v4, v115, 4, 5\n"
"v_lshrrev_b32 v5, 9, v115\n"
"v_lshlrev_b32 v4, 5, v4\n"
"v_lshl_add_u32 v4, v5, 2, v4\n"
"s_cmp_eq_u32 s32, 0\n"
"s_cselect_b32 s35, 0, 1073741824\n"
"v_or_b32 v4, s35, v4\n"
"v_lshrrev_b32 v5, 2, v115\n"
"v_add_u32 v5, 161792, v5\n"
"s_lshl_b32 s34, s32, 8\n"
"v_add_u32 v6, s34, v5\n"
"v_mov_b32 v8, 0\n"
"v_mov_b32 v9, 0\n"
"v_xor_b32 v94, 2048, v94\n"
"s_add_u32 s40, s40, 2097152\n"
"s_add_u32 s41, s41, 2097152\n"
"s_add_u32 s42, s42, 65536\n"
"s_add_u32 s43, s43, 65536\n"
"s_add_u32 s44, s44, 2097152\n"
"s_add_u32 s49, s44, 2097152\n"
"s_add_u32 s54, s46, 4194304\n"
"s_add_u32 s60, s47, 131072\n"
"s_mov_b32 s33, 2\n"
"s_cmp_eq_u32 s61, 3\n"
"s_cselect_b32 s54, 2, 1\n"
"s_lshl_b32 s54, s54, 21\n"
"s_add_u32 s54, s54, s46\n"
"s_waitcnt vmcnt(10)\n"
"ds_read_b32 v61, v56 offset:0\n"
"s_waitcnt lgkmcnt(0)\n"
"v_cmp_eq_u32 vcc, -1, v61\n"
"s_cbranch_vccnz L_os1\n"
"L_or1:\n"
"s_waitcnt lgkmcnt(0)\n"
"s_barrier\n"
"ds_read_b128 v[128:131], v116 offset:0\n"
"ds_read_b128 v[132:135], v116 offset:1024\n"
"ds_read_b128 v[136:139], v116 offset:2048\n"
"ds_read_b128 v[140:143], v116 offset:3072\n"
"v_mov_b32 v92, 0\n"
"s_waitcnt lgkmcnt(3)\n"
"v_mfma_f32_32x32x16_bf16 v[32:47], a[128:131], v[128:131], v[32:47]\n"
"ds_read_b128 v[144:147], v116 offset:4096\n"
"s_add_u32 m0, s48, 4096\n"
"v_add_u32 v119, 4096, v126\n"
"buffer_load_dwordx4 v119, s[8:11], 0 offen offset:2048 sc1 lds\n"
"s_waitcnt lgkmcnt(3)\n"
"v_mfma_f32_32x32x16_bf16 v[32:47], a[132:135], v[132:135], v[32:47]\n"
"ds_read_b128 v[148:151], v116 offset:5120\n"
"v_max3_u32 v92, v92, v128, v132\n"
"s_waitcnt lgkmcnt(3)\n"
"v_mfma_f32_32x32x16_bf16 v[32:47], a[136:139], v[136:139], v[32:47]\n"
"ds_read_b128 v[152:155], v116 offset:6144\n"
"s_waitcnt lgkmcnt(3)\n"
"v_mfma_f32_32x32x16_bf16 v[32:47], a[140:143], v[140:143], v[32:47]\n"
"ds_read_b128 v[156:159], v116 offset:7168\n"
"v_max3_u32 v92, v92, v136, v140\n"
"buffer_load_dwordx4 v119, s[8:11], 0 offen offset:3072 sc1 lds\n"
"s_waitcnt lgkmcnt(3)\n"
"v_mfma_f32_32x32x16_bf16 v[32:47], a[144:147], v[144:147], v[32:47]\n"
"ds_read_b128 v[160:163], v116 offset:8192\n"
"s_waitcnt lgkmcnt(3)\n"
"v_mfma_f32_32x32x16_bf16 v[32:47], a[148:151], v[148:151], v[32:47]\n"
"ds_read_b128 v[164:167], v116 offset:9216\n"
"v_max3_u32 v92, v92, v144, v148\n"
"s_waitcnt lgkmcnt(3)\n"
"v_mfma_f32_32x32x16_bf16 v[32:47], a[152:155], v[152:155], v[32:47]\n"
"ds_read_b128 v[168:171], v116 offset:10240\n"
"s_waitcnt lgkmcnt(3)\n"
"v_mfma_f32_32x32x16_bf16 v[32:47], a[156:159], v[156:159], v[32:47]\n"
"ds_read_b128 v[172:175], v116 offset:11264\n"
"v_max3_u32 v92, v92, v152, v156\n"
"s_add_u32 m0, s48, 32768\n"
"v_add_u32 v119, 32768, v126\n"
"buffer_load_dwordx4 v119, s[8:11], 0 offen offset:0 sc1 lds\n"
"s_waitcnt lgkmcnt(3)\n"
"v_mfma_f32_32x32x16_bf16 v[32:47], a[160:163], v[160:163], v[32:47]\n"
"ds_read_b128 v[176:179], v116 offset:12288\n"
"s_waitcnt lgkmcnt(3)\n"
"v_mfma_f32_32x32x16_bf16 v[32:47], a[164:167], v[164:167], v[32:47]\n"
"s_waitcnt vmcnt(9)\n"
"ds_read_b32 v61, v56 offset:16384\n"
"ds_read_b128 v[180:183], v116 offset:13312\n"
"v_max3_u32 v92, v92, v160, v164\n"
"s_waitcnt lgkmcnt(4)\n"
"v_mfma_f32_32x32x16_bf16 v[32:47], a[168:171], v[168:171], v[32:47]\n"
"ds_read_b128 v[184:187], v116 offset:14336\n"
"s_waitcnt lgkmcnt(4)\n"
"v_mfma_f32_32x32x16_bf16 v[32:47], a[172:175], v[172:175], v[32:47]\n"
"s_waitcnt lgkmcnt(2)\n"
"v_cmp_eq_u32 vcc, -1, v61\n"
"s_cbranch_vccnz L_ot1\n"
"L_ou1:\n"
"s_barrier\n"
"ds_read_b128 v[188:191], v116 offset:15360\n"
"v_max3_u32 v92, v92, v168, v172\n"
"buffer_load_dwordx4 v119, s[8:11], 0 offen offset:1024 sc1 lds\n"
"v_mfma_f32_32x32x16_bf16 v[32:47], a[176:179], v[176:179], v[32:47]\n"
"ds_read_b128 v[192:195], v116 offset:16384\n"
"s_waitcnt lgkmcnt(3)\n"
"v_mfma_f32_32x32x16_bf16 v[32:47], a[180:183], v[180:183], v[32:47]\n"
"ds_read_b128 v[196:199], v116 offset:17408\n"
"v_max3_u32 v92, v92, v176, v180\n"
"s_waitcnt lgkmcnt(3)\n"
"v_mfma_f32_32x32x16_bf16 v[32:47], a[184:187], v[184:187], v[32:47]\n"
"ds_read_b128 v[200:203], v116 offset:18432\n"
"s_waitcnt lgkmcnt(3)\n"
"v_mfma_f32_32x32x16_bf16 v[32:47], a[188:191], v[188:191], v[32:47]\n"
"ds_read_b128 v[204:207], v116 offset:19456\n"
"v_max3_u32 v92, v92, v184, v188\n"
"buffer_load_dwordx4 v119, s[8:11], 0 offen offset:2048 sc1 lds\n"
"s_waitcnt lgkmcnt(3)\n"
"v_mfma_f32_32x32x16_bf16 v[32:47], a[192:195], v[192:195], v[32:47]\n"
"ds_read_b128 v[208:211], v116 offset:20480\n"
"s_waitcnt lgkmcnt(3)\n"
"v_mfma_f32_32x32x16_bf16 v[32:47], a[196:199], v[196:199], v[32:47]\n"
"ds_read_b128 v[212:215], v116 offset:21504\n"
"v_max3_u32 v92, v92, v192, v196\n"
"s_waitcnt lgkmcnt(3)\n"
"v_mfma_f32_32x32x16_bf16 v[32:47], a[200:203], v[200:203], v[32:47]\n"
"ds_read_b128 v[216:219], v116 offset:22528\n"
"buffer_load_dwordx4 v119, s[8:11], 0 offen offset:3072 sc1 lds\n"
"s_waitcnt lgkmcnt(3)\n"
"v_mfma_f32_32x32x16_bf16 v[32:47], a[204:207], v[204:207], v[32:47]\n"
"ds_read_b128 v[220:223], v116 offset:23552\n"
"v_max3_u32 v92, v92, v200, v204\n"
"s_waitcnt lgkmcnt(3)\n"
"v_mfma_f32_32x32x16_bf16 v[32:47], a[208:211], v[208:211], v[32:47]\n"
"ds_read_b128 v[224:227], v116 offset:24576\n"
"s_waitcnt lgkmcnt(3)\n"
"v_mfma_f32_32x32x16_bf16 v[32:47], a[212:215], v[212:215], v[32:47]\n"
"ds_read_b128 v[228:231], v116 offset:25600\n"
"v_max3_u32 v92, v92, v208, v212\n"
"s_waitcnt lgkmcnt(3)\n"
"v_mfma_f32_32x32x16_bf16 v[32:47], a[216:219], v[216:219], v[32:47]\n"
"ds_read_b128 v[232:235], v116 offset:26624\n"
"s_add_u32 m0, s48, 36864\n"
"v_add_u32 v119, 36864, v126\n"
"buffer_load_dwordx4 v119, s[8:11], 0 offen offset:0 sc1 lds\n"
"s_waitcnt lgkmcnt(3)\n"
"v_mfma_f32_32x32x16_bf16 v[32:47], a[220:223], v[220:223], v[32:47]\n"
"ds_read_b128 v[236:239], v116 offset:27648\n"
"v_max3_u32 v92, v92, v216, v220\n"
"s_waitcnt lgkmcnt(3)\n"
"v_mfma_f32_32x32x16_bf16 v[32:47], a[224:227], v[224:227], v[32:47]\n"
"ds_read_b128 v[240:243], v116 offset:28672\n"
"s_waitcnt lgkmcnt(3)\n"
"v_mfma_f32_32x32x16_bf16 v[32:47], a[228:231], v[228:231], v[32:47]\n"
"ds_read_b128 v[244:247], v116 offset:29696\n"
"v_max3_u32 v92, v92, v224, v228\n"
"s_waitcnt lgkmcnt(3)\n"
"v_mfma_f32_32x32x16_bf16 v[32:47], a[232:235], v[232:235], v[32:47]\n"
"ds_read_b128 v[248:251], v116 offset:30720\n"
"buffer_load_dwordx4 v119, s[8:11], 0 offen offset:1024 sc1 lds\n"
"s_waitcnt lgkmcnt(3)\n"
"v_mfma_f32_32x32x16_bf16 v[32:47], a[236:239], v[236:239], v[32:47]\n"
"ds_read_b128 v[252:255], v116 offset:31744\n"
"v_max3_u32 v92, v92, v232, v236\n"
"s_waitcnt lgkmcnt(3)\n"
"v_mfma_f32_32x32x16_bf16 v[32:47], a[240:243], v[240:243], v[32:47]\n"
"s_waitcnt lgkmcnt(2)\n"
"v_mfma_f32_32x32x16_bf16 v[32:47], a[244:247], v[244:247], v[32:47]\n"
"v_max3_u32 v92, v92, v240, v244\n"
"s_waitcnt lgkmcnt(1)\n"
"v_mfma_f32_32x32x16_bf16 v[32:47], a[248:251], v[248:251], v[32:47]\n"
"s_waitcnt lgkmcnt(0)\n"
"v_mfma_f32_32x32x16_bf16 v[32:47], a[252:255], v[252:255], v[32:47]\n"
"v_max3_u32 v92, v92, v248, v252\n"
"v_cmp_eq_u32 vcc, -1, v92\n"
"s_cbranch_vccnz L_fail_o1\n"
"L_ok_o1:\n"
"s_nop 8\n"
"v_cvt_pk_bf16_f32 v104, v32, v33\n"
"v_cvt_pk_bf16_f32 v105, v34, v35\n"
"v_cvt_pk_bf16_f32 v106, v36, v37\n"
"v_cvt_pk_bf16_f32 v107, v38, v39\n"
"v_cvt_pk_bf16_f32 v108, v40, v41\n"
"v_cvt_pk_bf16_f32 v109, v42, v43\n"
"v_cvt_pk_bf16_f32 v110, v44, v45\n"
"v_cvt_pk_bf16_f32 v111, v46, v47\n"
"v_min_u32 v104, -2, v104\n"
"buffer_store_dwordx4 v[104:107], v115, s[8:11], s46 offen sc1\n"
"v_min_u32 v108, -2, v108\n"
"buffer_store_dwordx4 v[108:111], v115, s[8:11], s46 offen offset:1024 sc1\n"
"v_dot2c_f32_bf16 v8, s52, v104\n"
"v_dot2c_f32_bf16 v9, v104, v104\n"
"v_dot2c_f32_bf16 v8, s52, v105\n"
"v_dot2c_f32_bf16 v9, v105, v105\n"
"s_add_u32 m0, s48, 36864\n"
"v_add_u32 v119, 36864, v126\n"
"buffer_load_dwordx4 v119, s[8:11], 0 offen offset:2048 sc1 lds\n"
"v_dot2c_f32_bf16 v8, s52, v106\n"
"v_dot2c_f32_bf16 v9, v106, v106\n"
"v_dot2c_f32_bf16 v8, s52, v107\n"
"v_dot2c_f32_bf16 v9, v107, v107\n"
"v_dot2c_f32_bf16 v8, s52, v108\n"
"v_dot2c_f32_bf16 v9, v108, v108\n"
"buffer_load_dwordx4 v119, s[8:11], 0 offen offset:3072 sc1 lds\n"
"v_dot2c_f32_bf16 v8, s52, v109\n"
"v_dot2c_f32_bf16 v9, v109, v109\n"
"v_dot2c_f32_bf16 v8, s52, v110\n"
"v_dot2c_f32_bf16 v9, v110, v110\n"
"v_dot2c_f32_bf16 v8, s52, v111\n"
"v_dot2c_f32_bf16 v9, v111, v111\n"
"v_mov_b32 v40, v0\n"
"v_mov_b32 v41, v1\n"
"v_mov_b32 v42, v2\n"
"v_permlane32_swap_b32 v8, v9\n"
"v_add_f32 v8, v8, v9\n"
"ds_write_b32 v6, v8\n"
"v_mov_b32 v43, v3\n"
"v_mov_b32 v38, v115\n"
"v_mov_b32 v92, 1073741824\n"
"v_mov_b32 v39, 1073741824\n"
"s_cmp_eq_u32 s61, 3\n"
"s_cselect_b32 s34, 1073741824, 0\n"
"v_or_b32 v92, s34, v115\n"
"v_mov_b32 v44, -1\n"
"v_mov_b32 v45, -1\n"
"v_mov_b32 v46, -1\n"
"v_mov_b32 v47, -1\n"
"s_add_u32 s45, s45, 2097152\n"
"s_add_u32 s46, s46, 2097152\n"
"s_waitcnt lgkmcnt(0)\n"
"s_barrier\n"
"ds_read_b32 v10, v5 offset:0\n"
"ds_read_b32 v11, v5 offset:256\n"
"ds_read_b32 v12, v5 offset:512\n"
"ds_read_b32 v13, v5 offset:768\n"
"s_add_u32 m0, s48, 65536\n"
"v_add_u32 v119, 65536, v126\n"
"buffer_load_dwordx4 v119, s[8:11], 0 offen offset:0 sc1 lds\n"
"s_waitcnt lgkmcnt(2)\n"
"v_add_f32 v10, v10, v11\n"
"s_waitcnt lgkmcnt(0)\n"
"v_add_f32 v12, v12, v13\n"
"v_add_f32 v10, v10, v12\n"
"v_min_u32 v10, -2, v10\n"
"buffer_store_dword v10, v4, s[8:11], s47 offen sc1\n"
"s_add_u32 s47, s47, 65536\n"
"buffer_load_dwordx4 v119, s[8:11], 0 offen offset:1024 sc1 lds\n"
"buffer_load_dwordx4 v119, s[8:11], 0 offen offset:2048 sc1 lds\n"
"buffer_load_dwordx4 v119, s[8:11], 0 offen offset:3072 sc1 lds\n"
"s_add_u32 m0, s48, 69632\n"
"v_add_u32 v119, 69632, v126\n"
"buffer_load_dwordx4 v119, s[8:11], 0 offen offset:0 sc1 lds\n"
"buffer_load_dwordx4 v119, s[8:11], 0 offen offset:1024 sc1 lds\n"
"buffer_load_dwordx4 v119, s[8:11], 0 offen offset:2048 sc1 lds\n"
"buffer_load_dwordx4 v119, s[8:11], 0 offen offset:3072 sc1 lds\n"
"v_xor_b32 v126, 0x200000, v126\n"
"s_waitcnt vmcnt(11)\n"
"s_branch L_q\n"
"L_t2:\n"
"v_mfma_f32_16x16x32_bf16 v[88:91], v[36:39], v[100:103], v[88:91]\n"
"v_mfma_f32_32x32x16_bf16 v[16:31], v[40:43], v[96:99], v[16:31]\n"
"v_mfma_f32_32x32x16_bf16 v[0:15], v[44:47], v[96:99], v[0:15]\n"
"s_waitcnt lgkmcnt(4)\n"
"v_mfma_f32_32x32x16_bf16 v[32:47], v[128:131], v[64:67], 0\n"
"ds_read_b128 v[52:55], v113 offset:3072\n"
"s_nop 2\n"
"v_cvt_pk_bf16_f32 v88, v88, v89\n"
"v_cvt_pk_bf16_f32 v89, v90, v91\n"
"s_cmp_lt_u32 s32, 2\n"
"s_cbranch_scc1 L_hk18\n"
"ds_write_b16 v121, v88\n"
"ds_write_b16_d16_hi v121, v88 offset:16\n"
"ds_write_b16 v121, v89 offset:32\n"
"ds_write_b16_d16_hi v121, v89 offset:48\n"
"s_branch L_hd19\n"
"L_hk18:\n"
"ds_write_b64 v121, v[88:89]\n"
"L_hd19:\n"
"s_waitcnt lgkmcnt(4)\n"
"v_mfma_f32_32x32x16_bf16 v[32:47], v[132:135], v[68:71], v[32:47]\n"
"ds_read_b128 v[56:59], v113 offset:4096\n"
"v_cvt_pk_bf16_f32 v16, v16, v17\n"
"v_cvt_pk_bf16_f32 v17, v18, v19\n"
"v_cvt_pk_bf16_f32 v18, v20, v21\n"
"v_cvt_pk_bf16_f32 v19, v22, v23\n"
"v_cvt_pk_bf16_f32 v20, v24, v25\n"
"v_cvt_pk_bf16_f32 v21, v26, v27\n"
"v_cvt_pk_bf16_f32 v22, v28, v29\n"
"s_waitcnt lgkmcnt(4)\n"
"v_mfma_f32_32x32x16_bf16 v[32:47], v[136:139], v[48:51], v[32:47]\n"
"v_cvt_pk_bf16_f32 v23, v30, v31\n"
"ds_write_b128 v117, v[16:19] offset:35840\n"
"ds_write_b128 v117, v[20:23] offset:36864\n"
"ds_read_b128 v[48:51], v113 offset:5120\n"
"ds_read_b128 v[88:91], v95\n"
"v_cvt_pk_bf16_f32 v24, v0, v1\n"
"v_cvt_pk_bf16_f32 v25, v2, v3\n"
"s_waitcnt lgkmcnt(6)\n"
"v_mfma_f32_32x32x16_bf16 v[32:47], v[140:143], v[52:55], v[32:47]\n"
"v_cvt_pk_bf16_f32 v26, v4, v5\n"
"v_cvt_pk_bf16_f32 v27, v6, v7\n"
"v_cvt_pk_bf16_f32 v28, v8, v9\n"
"v_cvt_pk_bf16_f32 v29, v10, v11\n"
"v_cvt_pk_bf16_f32 v30, v12, v13\n"
"v_cvt_pk_bf16_f32 v31, v14, v15\n"
"v_mfma_f32_32x32x16_bf16 v[0:15], v[16:19], v[24:27], v[72:87]\n"
"s_waitcnt lgkmcnt(0)\n"
"s_barrier\n"
"v_mfma_f32_32x32x16_bf16 v[72:87], v[20:23], v[28:31], v[72:87]\n"
"ds_read_b128 v[64:67], v117 offset:32768\n"
"ds_read_b128 v[68:71], v117 offset:33792\n"
"ds_read_b128 v[52:55], v113 offset:6144\n"
"v_mfma_f32_32x32x16_bf16 v[32:47], v[144:147], v[56:59], v[32:47]\n"
"ds_read_b32 v60, v122\n"
"ds_read_b32 v61, v123\n"
"ds_read_b32 v62, v124\n"
"ds_read_b32 v63, v125\n"
"ds_read_b128 v[56:59], v113 offset:7168\n"
"v_mfma_f32_32x32x16_bf16 v[32:47], v[148:151], v[48:51], v[32:47]\n"
"ds_read_b128 v[48:51], v113 offset:8192\n"
"s_waitcnt lgkmcnt(6)\n"
"v_mfma_f32_32x32x16_bf16 v[32:47], v[152:155], v[52:55], v[32:47]\n"
"ds_read_b128 v[52:55], v113 offset:9216\n"
"v_mfma_f32_32x32x16_bf16 v[128:143], v[64:67], v[24:27], v[76:91]\n"
"s_waitcnt lgkmcnt(2)\n"
"v_mfma_f32_32x32x16_bf16 v[32:47], v[156:159], v[56:59], v[32:47]\n"
"ds_read_b128 v[56:59], v113 offset:10240\n"
"v_mul_f32 v16, v60, v93\n"
"v_mul_f32 v17, v61, v93\n"
"v_mul_f32 v18, v62, v93\n"
"v_mul_f32 v19, v63, v93\n"
"s_add_u32 m0, s48, 0\n"
"v_add_u32 v119, 0, v126\n"
"buffer_load_dwordx4 v119, s[8:11], 0 offen offset:0 sc1 lds\n"
"v_mfma_f32_32x32x16_bf16 v[144:159], v[68:71], v[28:31], v[76:91]\n"
"v_max3_f32 v20, v0, v4, v8\n"
"v_max3_f32 v21, v1, v5, v9\n"
"v_max3_f32 v22, v2, v6, v10\n"
"v_max3_f32 v23, v3, v7, v11\n"
"s_waitcnt lgkmcnt(2)\n"
"v_mfma_f32_32x32x16_bf16 v[32:47], v[160:163], v[48:51], v[32:47]\n"
"ds_read_b128 v[48:51], v113 offset:11264\n"
"v_max3_f32 v20, v20, v12, v140\n"
"v_max3_f32 v21, v21, v13, v141\n"
"v_max3_f32 v22, v22, v14, v142\n"
"v_max3_f32 v23, v23, v15, v143\n"
"v_mul_f32 v20, v20, v16\n"
"buffer_load_dwordx4 v119, s[8:11], 0 offen offset:1024 sc1 lds\n"
"s_waitcnt lgkmcnt(2)\n"
"v_mfma_f32_32x32x16_bf16 v[32:47], v[164:167], v[52:55], v[32:47]\n"
"ds_read_b128 v[52:55], v113 offset:12288\n"
"v_mul_f32 v21, v21, v17\n"
"v_mul_f32 v22, v22, v18\n"
"v_mul_f32 v23, v23, v19\n"
"v_max3_f32 v68, v20, v21, v22\n"
"s_waitcnt lgkmcnt(2)\n"
"v_mfma_f32_32x32x16_bf16 v[32:47], v[168:171], v[56:59], v[32:47]\n"
"ds_read_b128 v[56:59], v113 offset:13312\n"
"v_max_f32 v68, v68, v23\n"
"v_max3_f32 v64, v72, v76, v80\n"
"v_max3_f32 v65, v73, v77, v81\n"
"v_max3_f32 v66, v74, v78, v82\n"
"s_waitcnt lgkmcnt(2)\n"
"v_mfma_f32_32x32x16_bf16 v[32:47], v[172:175], v[48:51], v[32:47]\n"
"ds_read_b128 v[48:51], v113 offset:14336\n"
"ds_read_b128 v[160:163], v116 offset:58368\n"
"ds_read_b128 v[164:167], v116 offset:59392\n"
"ds_read_b128 v[168:171], v116 offset:60416\n"
"v_max3_f32 v67, v75, v79, v83\n"
"v_max3_f32 v64, v64, v84, v156\n"
"v_max3_f32 v65, v65, v85, v157\n"
"v_max3_f32 v66, v66, v86, v158\n"
"buffer_load_dwordx4 v119, s[8:11], 0 offen offset:2048 sc1 lds\n"
"s_waitcnt lgkmcnt(5)\n"
"v_mfma_f32_32x32x16_bf16 v[32:47], v[176:179], v[52:55], v[32:47]\n"
"ds_read_b128 v[52:55], v113 offset:15360\n"
"v_max3_f32 v67, v67, v87, v159\n"
"v_mul_f32 v64, v64, v16\n"
"v_mul_f32 v65, v65, v17\n"
"v_mul_f32 v66, v66, v18\n"
"s_waitcnt lgkmcnt(5)\n"
"v_mfma_f32_32x32x16_bf16 v[32:47], v[180:183], v[56:59], v[32:47]\n"
"ds_read_b128 v[56:59], v113 offset:16384\n"
"v_mul_f32 v67, v67, v19\n"
"v_max3_f32 v69, v64, v65, v66\n"
"v_max_f32 v69, v69, v67\n"
"s_nop 1\n"
"v_permlane32_swap_b32 v68, v69\n"
"buffer_load_dwordx4 v119, s[8:11], 0 offen offset:3072 sc1 lds\n"
"s_waitcnt lgkmcnt(5)\n"
"v_mfma_f32_32x32x16_bf16 v[32:47], v[184:187], v[48:51], v[32:47]\n"
"ds_read_b128 v[48:51], v113 offset:17408\n"
"v_max_f32 v68, v68, v69\n"
"v_mov_b32 v69, v68\n"
"s_nop 1\n"
"v_permlane32_swap_b32 v68, v69\n"
"v_sub_f32 v20, v20, v68\n"
"s_waitcnt lgkmcnt(2)\n"
"v_mfma_f32_32x32x16_bf16 v[32:47], v[188:191], v[52:55], v[32:47]\n"
"ds_read_b128 v[52:55], v113 offset:18432\n"
"v_sub_f32 v21, v21, v68\n"
"v_sub_f32 v22, v22, v68\n"
"v_sub_f32 v23, v23, v68\n"
"v_sub_f32 v64, v64, v69\n"
"s_waitcnt lgkmcnt(2)\n"
"v_mfma_f32_32x32x16_bf16 v[32:47], v[192:195], v[56:59], v[32:47]\n"
"ds_read_b128 v[56:59], v113 offset:19456\n"
"v_sub_f32 v65, v65, v69\n"
"v_sub_f32 v66, v66, v69\n"
"v_sub_f32 v67, v67, v69\n"
"v_exp_f32 v20, v20\n"
"v_exp_f32 v21, v21\n"
"s_add_u32 m0, s48, 4096\n"
"v_add_u32 v119, 4096, v126\n"
"buffer_load_dwordx4 v119, s[8:11], 0 offen offset:0 sc1 lds\n"
"s_waitcnt lgkmcnt(2)\n"
"v_mfma_f32_32x32x16_bf16 v[32:47], v[196:199], v[48:51], v[32:47]\n"
"ds_read_b128 v[48:51], v113 offset:20480\n"
"v_exp_f32 v22, v22\n"
"v_exp_f32 v23, v23\n"
"v_exp_f32 v64, v64\n"
"v_exp_f32 v65, v65\n"
"s_waitcnt lgkmcnt(2)\n"
"v_mfma_f32_32x32x16_bf16 v[32:47], v[200:203], v[52:55], v[32:47]\n"
"ds_read_b128 v[52:55], v113 offset:21504\n"
"ds_read_b128 v[196:199], v94 offset:1024\n"
"v_exp_f32 v66, v66\n"
"v_exp_f32 v67, v67\n"
"v_add_f32 v68, v20, v21\n"
"v_add_f32 v70, v22, v23\n"
"buffer_load_dwordx4 v119, s[8:11], 0 offen offset:1024 sc1 lds\n"
"s_waitcnt lgkmcnt(3)\n"
"v_mfma_f32_32x32x16_bf16 v[32:47], v[204:207], v[56:59], v[32:47]\n"
"ds_read_b128 v[56:59], v113 offset:22528\n"
"v_add_f32 v69, v64, v65\n"
"v_add_f32 v71, v66, v67\n"
"v_add_f32 v68, v68, v70\n"
"v_add_f32 v69, v69, v71\n"
"s_waitcnt lgkmcnt(3)\n"
"v_mfma_f32_32x32x16_bf16 v[32:47], v[208:211], v[48:51], v[32:47]\n"
"ds_read_b128 v[48:51], v113 offset:23552\n"
"v_mov_b32 v180, 0\n"
"v_mov_b32 v181, 0\n"
"v_mov_b32 v192, 0\n"
"v_mov_b32 v193, 0\n"
"s_waitcnt lgkmcnt(3)\n"
"v_mfma_f32_32x32x16_bf16 v[32:47], v[212:215], v[52:55], v[32:47]\n"
"ds_read_b128 v[52:55], v113 offset:24576\n"
"v_mul_f32 v20, v20, v60\n"
"v_mul_f32 v21, v21, v61\n"
"v_permlane32_swap_b32 v68, v69\n"
"v_add_f32 v68, v68, v69\n"
"buffer_load_dwordx4 v119, s[8:11], 0 offen offset:2048 sc1 lds\n"
"s_waitcnt lgkmcnt(2)\n"
"v_mfma_f32_32x32x16_bf16 v[32:47], v[216:219], v[56:59], v[32:47]\n"
"ds_read_b128 v[56:59], v113 offset:25600\n"
"v_rcp_f32 v68, v68\n"
"v_mul_f32 v22, v22, v62\n"
"v_mov_b32 v69, v68\n"
"v_mul_f32 v23, v23, v63\n"
"s_waitcnt lgkmcnt(2)\n"
"v_mfma_f32_32x32x16_bf16 v[32:47], v[220:223], v[48:51], v[32:47]\n"
"ds_read_b128 v[48:51], v113 offset:26624\n"
"v_mul_f32 v64, v64, v60\n"
"v_permlane32_swap_b32 v68, v69\n"
"v_mul_f32 v65, v65, v61\n"
"v_mul_f32 v66, v66, v62\n"
"buffer_load_dwordx4 v119, s[8:11], 0 offen offset:3072 sc1 lds\n"
"s_waitcnt lgkmcnt(2)\n"
"v_mfma_f32_32x32x16_bf16 v[32:47], v[224:227], v[52:55], v[32:47]\n"
"ds_read_b128 v[52:55], v113 offset:27648\n"
"v_mul_f32 v67, v67, v63\n"
"v_mul_f32 v20, v20, v68\n"
"v_mul_f32 v21, v21, v68\n"
"v_mul_f32 v22, v22, v68\n"
"v_mul_f32 v23, v23, v68\n"
"s_waitcnt lgkmcnt(2)\n"
"v_mfma_f32_32x32x16_bf16 v[32:47], v[228:231], v[56:59], v[32:47]\n"
"ds_read_b128 v[56:59], v113 offset:28672\n"
"v_cvt_pk_bf16_f32 v20, v20, v21\n"
"v_cvt_pk_bf16_f32 v21, v22, v23\n"
"v_and_b32 v172, v20, v162\n"
"v_and_b32 v173, v21, v163\n"
"s_waitcnt lgkmcnt(2)\n"
"v_mfma_f32_32x32x16_bf16 v[32:47], v[232:235], v[48:51], v[32:47]\n"
"ds_read_b128 v[48:51], v113 offset:29696\n"
"v_and_b32 v174, v20, v164\n"
"v_and_b32 v175, v21, v165\n"
"v_and_b32 v176, v20, v166\n"
"v_and_b32 v177, v21, v167\n"
"s_add_u32 m0, s48, 32768\n"
"v_add_u32 v119, 32768, v126\n"
"buffer_load_dwordx4 v119, s[8:11], 0 offen offset:0 sc1 lds\n"
"s_waitcnt lgkmcnt(2)\n"
"v_mfma_f32_32x32x16_bf16 v[32:47], v[236:239], v[52:55], v[32:47]\n"
"ds_read_b128 v[52:55], v113 offset:30720\n"
"v_and_b32 v178, v20, v168\n"
"v_and_b32 v179, v21, v169\n"
"v_and_b32 v182, v20, v160\n"
"v_and_b32 v183, v21, v161\n"
"s_waitcnt lgkmcnt(2)\n"
"v_mfma_f32_32x32x16_bf16 v[32:47], v[240:243], v[56:59], v[32:47]\n"
"ds_read_b128 v[56:59], v113 offset:31744\n"
"v_mul_f32 v64, v64, v69\n"
"v_mul_f32 v65, v65, v69\n"
"v_mul_f32 v66, v66, v69\n"
"v_mul_f32 v67, v67, v69\n"
"buffer_load_dwordx4 v119, s[8:11], 0 offen offset:1024 sc1 lds\n"
"s_waitcnt lgkmcnt(2)\n"
"v_mfma_f32_32x32x16_bf16 v[32:47], v[244:247], v[48:51], v[32:47]\n"
"v_cvt_pk_bf16_f32 v64, v64, v65\n"
"v_cvt_pk_bf16_f32 v65, v66, v67\n"
"v_and_b32 v184, v64, v162\n"
"v_and_b32 v185, v65, v163\n"
"s_waitcnt lgkmcnt(1)\n"
"v_mfma_f32_32x32x16_bf16 v[32:47], v[248:251], v[52:55], v[32:47]\n"
"v_and_b32 v186, v64, v164\n"
"v_and_b32 v187, v65, v165\n"
"v_and_b32 v188, v64, v166\n"
"v_and_b32 v189, v65, v167\n"
"s_waitcnt lgkmcnt(0)\n"
"v_mfma_f32_32x32x16_bf16 v[32:47], v[252:255], v[56:59], v[32:47]\n"
"v_and_b32 v190, v64, v168\n"
"v_and_b32 v191, v65, v169\n"
"v_and_b32 v194, v64, v160\n"
"v_and_b32 v195, v65, v161\n"
"v_mfma_f32_32x32x16_bf16 v[32:47], v[96:99], v[196:199], v[32:47]\n"
"s_nop 11\n"
"v_cvt_pk_bf16_f32 v40, v40, v41\n"
"v_cvt_pk_bf16_f32 v41, v42, v43\n"
"v_cvt_pk_bf16_f32 v42, v44, v45\n"
"v_cvt_pk_bf16_f32 v43, v46, v47\n"
"ds_write_b128 v117, v[40:43] offset:37888\n"
"v_cvt_pk_bf16_f32 v32, v32, v33\n"
"v_mfma_f32_32x32x16_bf16 v[0:15], v[40:43], v[176:179], 0\n"
"v_cvt_pk_bf16_f32 v33, v34, v35\n"
"v_cvt_pk_bf16_f32 v34, v36, v37\n"
"v_cvt_pk_bf16_f32 v35, v38, v39\n"
"v_mfma_f32_32x32x16_bf16 v[16:31], v[40:43], v[188:191], 0\n"
"s_waitcnt vmcnt(11)\n"
"s_waitcnt lgkmcnt(0)\n"
"s_barrier\n"
"ds_read_b128 v[44:47], v117 offset:34816\n"
"v_mfma_f32_32x32x16_bf16 v[0:15], v[32:35], v[172:175], v[0:15]\n"
"v_mfma_f32_32x32x16_bf16 v[16:31], v[32:35], v[184:187], v[16:31]\n"
"s_waitcnt lgkmcnt(0)\n"
"v_mfma_f32_32x32x16_bf16 v[0:15], v[44:47], v[180:183], v[0:15]\n"
"v_mfma_f32_32x32x16_bf16 v[16:31], v[44:47], v[192:195], v[16:31]\n"
"s_nop 10\n"
"v_cvt_pk_bf16_f32 v48, v0, v1\n"
"v_cvt_pk_bf16_f32 v49, v2, v3\n"
"v_cvt_pk_bf16_f32 v50, v4, v5\n"
"v_cvt_pk_bf16_f32 v51, v6, v7\n"
"v_min_u32 v48, -2, v48\n"
"buffer_store_dwordx4 v[48:51], v115, s[8:11], s44 offen sc1\n"
"v_cvt_pk_bf16_f32 v52, v24, v25\n"
"v_cvt_pk_bf16_f32 v53, v26, v27\n"
"v_cvt_pk_bf16_f32 v54, v28, v29\n"
"v_cvt_pk_bf16_f32 v55, v30, v31\n"
"v_min_u32 v52, -2, v52\n"
"buffer_store_dwordx4 v[52:55], v115, s[8:11], s44 offen offset:1024 sc1\n"
"s_add_u32 m0, s48, 32768\n"
"v_add_u32 v119, 32768, v126\n"
"buffer_load_dwordx4 v119, s[8:11], 0 offen offset:2048 sc1 lds\n"
"buffer_load_dwordx4 v119, s[8:11], 0 offen offset:3072 sc1 lds\n"
"s_add_u32 m0, s48, 36864\n"
"v_add_u32 v119, 36864, v126\n"
"buffer_load_dwordx4 v119, s[8:11], 0 offen offset:0 sc1 lds\n"
"buffer_load_dwordx4 v119, s[8:11], 0 offen offset:1024 sc1 lds\n"
"v_lshlrev_b32 v32, 16, v104\n"
"v_and_b32 v33, 0xffff0000, v104\n"
"v_lshlrev_b32 v34, 16, v105\n"
"v_and_b32 v35, 0xffff0000, v105\n"
"v_lshlrev_b32 v36, 16, v106\n"
"v_and_b32 v37, 0xffff0000, v106\n"
"v_lshlrev_b32 v38, 16, v107\n"
"v_and_b32 v39, 0xffff0000, v107\n"
"v_lshlrev_b32 v40, 16, v108\n"
"v_and_b32 v41, 0xffff0000, v108\n"
"v_lshlrev_b32 v42, 16, v109\n"
"v_and_b32 v43, 0xffff0000, v109\n"
"v_lshlrev_b32 v44, 16, v110\n"
"v_and_b32 v45, 0xffff0000, v110\n"
"v_lshlrev_b32 v46, 16, v111\n"
"v_and_b32 v47, 0xffff0000, v111\n"
"v_lshrrev_b32 v56, 2, v115\n"
"v_and_b32 v56, 0x30, v56\n"
"v_lshl_add_u32 v56, v115, 2, v56\n"
"v_add_u32 v56, s51, v56\n"
"v_lshrrev_b32 v12, 9, v115\n"
"v_bfe_u32 v13, v115, 8, 1\n"
"v_lshl_add_u32 v12, v12, 1, v13\n"
"v_bfe_u32 v13, v115, 7, 1\n"
"v_and_b32 v14, 0x70, v115\n"
"v_lshl_add_u32 v14, v13, 9, v14\n"
"v_lshl_add_u32 v12, v12, 10, v14\n"
"s_cmp_gt_u32 s59, 0\n"
"s_cselect_b32 s34, 384, 1073741824\n"
"s_cselect_b32 s35, 8, 99\n"
"v_add_u32 v0, s34, v12\n"
"v_bfe_u32 v12, v115, 4, 5\n"
"v_lshrrev_b32 v13, 9, v115\n"
"v_lshlrev_b32 v12, 5, v12\n"
"v_lshl_add_u32 v1, v13, 4, v12\n"
"v_bfe_u32 v12, v115, 4, 4\n"
"v_lshrrev_b32 v13, 8, v115\n"
"v_cmp_lt_u32 vcc, s35, v12\n"
"v_lshlrev_b32 v12, 5, v12\n"
"v_lshl_add_u32 v12, v13, 3, v12\n"
"v_add_u32 v12, 512, v12\n"
"v_mov_b32 v13, 1073741824\n"
"v_cndmask_b32 v2, v13, v12\n"
"v_add_u32 v3, s50, v115\n"
"v_bfe_u32 v4, v115, 4, 5\n"
"v_lshrrev_b32 v5, 9, v115\n"
"v_lshlrev_b32 v4, 5, v4\n"
"v_lshl_add_u32 v4, v5, 2, v4\n"
"s_cmp_eq_u32 s32, 0\n"
"s_cselect_b32 s35, 0, 1073741824\n"
"v_or_b32 v4, s35, v4\n"
"v_lshrrev_b32 v5, 2, v115\n"
"v_add_u32 v5, 161792, v5\n"
"s_lshl_b32 s34, s32, 8\n"
"v_add_u32 v6, s34, v5\n"
"v_mov_b32 v8, 0\n"
"v_mov_b32 v9, 0\n"
"v_xor_b32 v94, 2048, v94\n"
"s_add_u32 s40, s40, 2097152\n"
"s_add_u32 s41, s41, 2097152\n"
"s_add_u32 s42, s42, 65536\n"
"s_add_u32 s43, s43, 65536\n"
"s_add_u32 s44, s44, 2097152\n"
"s_add_u32 s49, s44, 2097152\n"
"s_add_u32 s54, s46, 4194304\n"
"s_add_u32 s60, s47, 131072\n"
"s_mov_b32 s33, 3\n"
"s_cmp_eq_u32 s61, 3\n"
"s_cselect_b32 s54, 2, 1\n"
"s_lshl_b32 s54, s54, 21\n"
"s_add_u32 s54, s54, s46\n"
"s_waitcnt vmcnt(4)\n"
"s_sleep 4\n"
"s_mov_b32 m0, s51\n"
"s_nop 0\n"
"buffer_load_dwordx4 v115, s[8:11], s45 offen offset:0 sc1 lds\n"
"buffer_load_dwordx4 v115, s[8:11], s45 offen offset:1024 sc1 lds\n"
"buffer_load_dwordx4 v115, s[8:11], s45 offen offset:2048 sc1 lds\n"
"buffer_load_dwordx4 v115, s[8:11], s45 offen offset:3072 sc1 lds\n"
"s_add_u32 m0, s51, 16384\n"
"s_add_u32 s35, s45, 16384\n"
"buffer_load_dwordx4 v115, s[8:11], s35 offen offset:0 sc1 lds\n"
"buffer_load_dwordx4 v115, s[8:11], s35 offen offset:1024 sc1 lds\n"
"buffer_load_dwordx4 v115, s[8:11], s35 offen offset:2048 sc1 lds\n"
"buffer_load_dwordx4 v115, s[8:11], s35 offen offset:3072 sc1 lds\n"
"s_add_u32 m0, s48, 36864\n"
"v_add_u32 v119, 36864, v126\n"
"buffer_load_dwordx4 v119, s[8:11], 0 offen offset:2048 sc1 lds\n"
"buffer_load_dwordx4 v119, s[8:11], 0 offen offset:3072 sc1 lds\n"
"s_add_u32 m0, s48, 65536\n"
"v_add_u32 v119, 65536, v126\n"
"buffer_load_dwordx4 v119, s[8:11], 0 offen offset:0 sc1 lds\n"
"buffer_load_dwordx4 v119, s[8:11], 0 offen offset:1024 sc1 lds\n"
"s_waitcnt vmcnt(8)\n"
"ds_read_b32 v61, v56 offset:0\n"
"s_waitcnt lgkmcnt(0)\n"
"v_cmp_eq_u32 vcc, -1, v61\n"
"s_cbranch_vccnz L_os2\n"
"L_or2:\n"
"s_waitcnt lgkmcnt(0)\n"
"s_barrier\n"
"ds_read_b128 v[128:131], v116 offset:0\n"
"ds_read_b128 v[132:135], v116 offset:1024\n"
"ds_read_b128 v[136:139], v116 offset:2048\n"
"ds_read_b128 v[140:143], v116 offset:3072\n"
"v_mov_b32 v92, 0\n"
"s_waitcnt lgkmcnt(3)\n"
"v_mfma_f32_32x32x16_bf16 v[32:47], a[0:3], v[128:131], v[32:47]\n"
"ds_read_b128 v[144:147], v116 offset:4096\n"
"s_add_u32 m0, s48, 65536\n"
"v_add_u32 v119, 65536, v126\n"
"buffer_load_dwordx4 v119, s[8:11], 0 offen offset:2048 sc1 lds\n"
"s_waitcnt lgkmcnt(3)\n"
"v_mfma_f32_32x32x16_bf16 v[32:47], a[4:7], v[132:135], v[32:47]\n"
"ds_read_b128 v[148:151], v116 offset:5120\n"
"v_max3_u32 v92, v92, v128, v132\n"
"s_waitcnt lgkmcnt(3)\n"
"v_mfma_f32_32x32x16_bf16 v[32:47], a[8:11], v[136:139], v[32:47]\n"
"ds_read_b128 v[152:155], v116 offset:6144\n"
"s_waitcnt lgkmcnt(3)\n"
"v_mfma_f32_32x32x16_bf16 v[32:47], a[12:15], v[140:143], v[32:47]\n"
"ds_read_b128 v[156:159], v116 offset:7168\n"
"v_max3_u32 v92, v92, v136, v140\n"
"s_waitcnt lgkmcnt(3)\n"
"v_mfma_f32_32x32x16_bf16 v[32:47], a[16:19], v[144:147], v[32:47]\n"
"ds_read_b128 v[160:163], v116 offset:8192\n"
"s_waitcnt lgkmcnt(3)\n"
"v_mfma_f32_32x32x16_bf16 v[32:47], a[20:23], v[148:151], v[32:47]\n"
"ds_read_b128 v[164:167], v116 offset:9216\n"
"v_max3_u32 v92, v92, v144, v148\n"
"buffer_load_dwordx4 v119, s[8:11], 0 offen offset:3072 sc1 lds\n"
"s_waitcnt lgkmcnt(3)\n"
"v_mfma_f32_32x32x16_bf16 v[32:47], a[24:27], v[152:155], v[32:47]\n"
"ds_read_b128 v[168:171], v116 offset:10240\n"
"s_waitcnt lgkmcnt(3)\n"
"v_mfma_f32_32x32x16_bf16 v[32:47], a[28:31], v[156:159], v[32:47]\n"
"ds_read_b128 v[172:175], v116 offset:11264\n"
"v_max3_u32 v92, v92, v152, v156\n"
"s_waitcnt lgkmcnt(3)\n"
"v_mfma_f32_32x32x16_bf16 v[32:47], a[32:35], v[160:163], v[32:47]\n"
"ds_read_b128 v[176:179], v116 offset:12288\n"
"s_waitcnt lgkmcnt(3)\n"
"v_mfma_f32_32x32x16_bf16 v[32:47], a[36:39], v[164:167], v[32:47]\n"
"s_waitcnt vmcnt(6)\n"
"ds_read_b32 v61, v56 offset:16384\n"
"ds_read_b128 v[180:183], v116 offset:13312\n"
"v_max3_u32 v92, v92, v160, v164\n"
"s_waitcnt lgkmcnt(4)\n"
"v_mfma_f32_32x32x16_bf16 v[32:47], a[40:43], v[168:171], v[32:47]\n"
"ds_read_b128 v[184:187], v116 offset:14336\n"
"s_add_u32 m0, s48, 69632\n"
"v_add_u32 v119, 69632, v126\n"
"buffer_load_dwordx4 v119, s[8:11], 0 offen offset:0 sc1 lds\n"
"s_waitcnt lgkmcnt(4)\n"
"v_mfma_f32_32x32x16_bf16 v[32:47], a[44:47], v[172:175], v[32:47]\n"
"s_waitcnt lgkmcnt(2)\n"
"v_cmp_eq_u32 vcc, -1, v61\n"
"s_cbranch_vccnz L_ot2\n"
"L_ou2:\n"
"s_barrier\n"
"ds_read_b128 v[188:191], v116 offset:15360\n"
"v_max3_u32 v92, v92, v168, v172\n"
"v_mfma_f32_32x32x16_bf16 v[32:47], a[48:51], v[176:179], v[32:47]\n"
"ds_read_b128 v[192:195], v116 offset:16384\n"
"s_waitcnt lgkmcnt(3)\n"
"v_mfma_f32_32x32x16_bf16 v[32:47], a[52:55], v[180:183], v[32:47]\n"
"ds_read_b128 v[196:199], v116 offset:17408\n"
"v_max3_u32 v92, v92, v176, v180\n"
"s_waitcnt lgkmcnt(3)\n"
"v_mfma_f32_32x32x16_bf16 v[32:47], a[56:59], v[184:187], v[32:47]\n"
"ds_read_b128 v[200:203], v116 offset:18432\n"
"s_waitcnt lgkmcnt(3)\n"
"v_mfma_f32_32x32x16_bf16 v[32:47], a[60:63], v[188:191], v[32:47]\n"
"ds_read_b128 v[204:207], v116 offset:19456\n"
"v_max3_u32 v92, v92, v184, v188\n"
"buffer_load_dwordx4 v119, s[8:11], 0 offen offset:1024 sc1 lds\n"
"s_waitcnt lgkmcnt(3)\n"
"v_mfma_f32_32x32x16_bf16 v[32:47], a[64:67], v[192:195], v[32:47]\n"
"ds_read_b128 v[208:211], v116 offset:20480\n"
"s_waitcnt lgkmcnt(3)\n"
"v_mfma_f32_32x32x16_bf16 v[32:47], a[68:71], v[196:199], v[32:47]\n"
"ds_read_b128 v[212:215], v116 offset:21504\n"
"v_max3_u32 v92, v92, v192, v196\n"
"s_waitcnt lgkmcnt(3)\n"
"v_mfma_f32_32x32x16_bf16 v[32:47], a[72:75], v[200:203], v[32:47]\n"
"ds_read_b128 v[216:219], v116 offset:22528\n"
"s_waitcnt lgkmcnt(3)\n"
"v_mfma_f32_32x32x16_bf16 v[32:47], a[76:79], v[204:207], v[32:47]\n"
"ds_read_b128 v[220:223], v116 offset:23552\n"
"v_max3_u32 v92, v92, v200, v204\n"
"s_waitcnt lgkmcnt(3)\n"
"v_mfma_f32_32x32x16_bf16 v[32:47], a[80:83], v[208:211], v[32:47]\n"
"ds_read_b128 v[224:227], v116 offset:24576\n"
"buffer_load_dwordx4 v119, s[8:11], 0 offen offset:2048 sc1 lds\n"
"s_waitcnt lgkmcnt(3)\n"
"v_mfma_f32_32x32x16_bf16 v[32:47], a[84:87], v[212:215], v[32:47]\n"
"ds_read_b128 v[228:231], v116 offset:25600\n"
"v_max3_u32 v92, v92, v208, v212\n"
"s_waitcnt lgkmcnt(3)\n"
"v_mfma_f32_32x32x16_bf16 v[32:47], a[88:91], v[216:219], v[32:47]\n"
"ds_read_b128 v[232:235], v116 offset:26624\n"
"s_waitcnt lgkmcnt(3)\n"
"v_mfma_f32_32x32x16_bf16 v[32:47], a[92:95], v[220:223], v[32:47]\n"
"ds_read_b128 v[236:239], v116 offset:27648\n"
"v_max3_u32 v92, v92, v216, v220\n"
"s_waitcnt lgkmcnt(3)\n"
"v_mfma_f32_32x32x16_bf16 v[32:47], a[96:99], v[224:227], v[32:47]\n"
"ds_read_b128 v[240:243], v116 offset:28672\n"
"s_waitcnt lgkmcnt(3)\n"
"v_mfma_f32_32x32x16_bf16 v[32:47], a[100:103], v[228:231], v[32:47]\n"
"ds_read_b128 v[244:247], v116 offset:29696\n"
"v_max3_u32 v92, v92, v224, v228\n"
"buffer_load_dwordx4 v119, s[8:11], 0 offen offset:3072 sc1 lds\n"
"s_waitcnt lgkmcnt(3)\n"
"v_mfma_f32_32x32x16_bf16 v[32:47], a[104:107], v[232:235], v[32:47]\n"
"ds_read_b128 v[248:251], v116 offset:30720\n"
"s_waitcnt lgkmcnt(3)\n"
"v_mfma_f32_32x32x16_bf16 v[32:47], a[108:111], v[236:239], v[32:47]\n"
"ds_read_b128 v[252:255], v116 offset:31744\n"
"v_max3_u32 v92, v92, v232, v236\n"
"s_waitcnt lgkmcnt(3)\n"
"v_mfma_f32_32x32x16_bf16 v[32:47], a[112:115], v[240:243], v[32:47]\n"
"s_waitcnt lgkmcnt(2)\n"
"v_mfma_f32_32x32x16_bf16 v[32:47], a[116:119], v[244:247], v[32:47]\n"
"v_max3_u32 v92, v92, v240, v244\n"
"s_waitcnt lgkmcnt(1)\n"
"v_mfma_f32_32x32x16_bf16 v[32:47], a[120:123], v[248:251], v[32:47]\n"
"s_waitcnt lgkmcnt(0)\n"
"v_mfma_f32_32x32x16_bf16 v[32:47], a[124:127], v[252:255], v[32:47]\n"
"v_max3_u32 v92, v92, v248, v252\n"
"v_cmp_eq_u32 vcc, -1, v92\n"
"s_cbranch_vccnz L_fail_o2\n"
"L_ok_o2:\n"
"s_nop 8\n"
"v_cvt_pk_bf16_f32 v104, v32, v33\n"
"v_cvt_pk_bf16_f32 v105, v34, v35\n"
"v_cvt_pk_bf16_f32 v106, v36, v37\n"
"v_cvt_pk_bf16_f32 v107, v38, v39\n"
"v_cvt_pk_bf16_f32 v108, v40, v41\n"
"v_cvt_pk_bf16_f32 v109, v42, v43\n"
"v_cvt_pk_bf16_f32 v110, v44, v45\n"
"v_cvt_pk_bf16_f32 v111, v46, v47\n"
"v_min_u32 v104, -2, v104\n"
"buffer_store_dwordx4 v[104:107], v115, s[8:11], s46 offen sc1\n"
"v_min_u32 v108, -2, v108\n"
"buffer_store_dwordx4 v[108:111], v115, s[8:11], s46 offen offset:1024 sc1\n"
"v_dot2c_f32_bf16 v8, s52, v104\n"
"v_dot2c_f32_bf16 v9, v104, v104\n"
"v_dot2c_f32_bf16 v8, s52, v105\n"
"v_dot2c_f32_bf16 v9, v105, v105\n"
"v_dot2c_f32_bf16 v8, s52, v106\n"
"v_dot2c_f32_bf16 v9, v106, v106\n"
"v_dot2c_f32_bf16 v8, s52, v107\n"
"v_dot2c_f32_bf16 v9, v107, v107\n"
"v_dot2c_f32_bf16 v8, s52, v108\n"
"v_dot2c_f32_bf16 v9, v108, v108\n"
"v_dot2c_f32_bf16 v8, s52, v109\n"
"v_dot2c_f32_bf16 v9, v109, v109\n"
"v_dot2c_f32_bf16 v8, s52, v110\n"
"v_dot2c_f32_bf16 v9, v110, v110\n"
"v_dot2c_f32_bf16 v8, s52, v111\n"
"v_dot2c_f32_bf16 v9, v111, v111\n"
"v_mov_b32 v40, v0\n"
"v_mov_b32 v41, v1\n"
"v_mov_b32 v42, v2\n"
"v_permlane32_swap_b32 v8, v9\n"
"v_add_f32 v8, v8, v9\n"
"ds_write_b32 v6, v8\n"
"v_mov_b32 v43, v3\n"
"v_mov_b32 v38, 1073741824\n"
"v_mov_b32 v92, 1073741824\n"
"v_mov_b32 v39, 1073741824\n"
"v_mov_b32 v44, -1\n"
"v_mov_b32 v45, -1\n"
"v_mov_b32 v46, -1\n"
"v_mov_b32 v47, -1\n"
"s_add_u32 s45, s45, 2097152\n"
"s_add_u32 s46, s46, 2097152\n"
"s_waitcnt lgkmcnt(0)\n"
"s_barrier\n"
"ds_read_b32 v10, v5 offset:0\n"
"ds_read_b32 v11, v5 offset:256\n"
"ds_read_b32 v12, v5 offset:512\n"
"ds_read_b32 v13, v5 offset:768\n"
"s_waitcnt lgkmcnt(2)\n"
"v_add_f32 v10, v10, v11\n"
"s_waitcnt lgkmcnt(0)\n"
"v_add_f32 v12, v12, v13\n"
"v_add_f32 v10, v10, v12\n"
"v_min_u32 v10, -2, v10\n"
"buffer_store_dword v10, v4, s[8:11], s47 offen sc1\n"
"s_add_u32 s47, s47, 65536\n"
"v_xor_b32 v126, 0x200000, v126\n"
"s_waitcnt vmcnt(1)\n"
"s_branch L_q\n"
"L_t3:\n"
"v_mfma_f32_16x16x32_bf16 v[88:91], v[36:39], v[100:103], v[88:91]\n"
"v_mfma_f32_32x32x16_bf16 v[16:31], v[40:43], v[96:99], v[16:31]\n"
"v_mfma_f32_32x32x16_bf16 v[0:15], v[44:47], v[96:99], v[0:15]\n"
"s_waitcnt lgkmcnt(4)\n"
"v_mfma_f32_32x32x16_bf16 v[32:47], v[128:131], v[64:67], 0\n"
"ds_read_b128 v[52:55], v113 offset:3072\n"
"s_nop 2\n"
"v_cvt_pk_bf16_f32 v88, v88, v89\n"
"v_cvt_pk_bf16_f32 v89, v90, v91\n"
"s_cmp_lt_u32 s32, 2\n"
"s_cbranch_scc1 L_hk20\n"
"ds_write_b16 v121, v88\n"
"ds_write_b16_d16_hi v121, v88 offset:16\n"
"ds_write_b16 v121, v89 offset:32\n"
"ds_write_b16_d16_hi v121, v89 offset:48\n"
"s_branch L_hd21\n"
"L_hk20:\n"
"ds_write_b64 v121, v[88:89]\n"
"L_hd21:\n"
"s_waitcnt lgkmcnt(4)\n"
"v_mfma_f32_32x32x16_bf16 v[32:47], v[132:135], v[68:71], v[32:47]\n"
"ds_read_b128 v[56:59], v113 offset:4096\n"
"v_cvt_pk_bf16_f32 v16, v16, v17\n"
"v_cvt_pk_bf16_f32 v17, v18, v19\n"
"v_cvt_pk_bf16_f32 v18, v20, v21\n"
"v_cvt_pk_bf16_f32 v19, v22, v23\n"
"v_cvt_pk_bf16_f32 v20, v24, v25\n"
"v_cvt_pk_bf16_f32 v21, v26, v27\n"
"v_cvt_pk_bf16_f32 v22, v28, v29\n"
"s_waitcnt lgkmcnt(4)\n"
"v_mfma_f32_32x32x16_bf16 v[32:47], v[136:139], v[48:51], v[32:47]\n"
"v_cvt_pk_bf16_f32 v23, v30, v31\n"
"ds_write_b128 v117, v[16:19] offset:35840\n"
"ds_write_b128 v117, v[20:23] offset:36864\n"
"ds_read_b128 v[48:51], v113 offset:5120\n"
"ds_read_b128 v[88:91], v95\n"
"v_cvt_pk_bf16_f32 v24, v0, v1\n"
"v_cvt_pk_bf16_f32 v25, v2, v3\n"
"s_waitcnt lgkmcnt(6)\n"
"v_mfma_f32_32x32x16_bf16 v[32:47], v[140:143], v[52:55], v[32:47]\n"
"v_cvt_pk_bf16_f32 v26, v4, v5\n"
"v_cvt_pk_bf16_f32 v27, v6, v7\n"
"v_cvt_pk_bf16_f32 v28, v8, v9\n"
"v_cvt_pk_bf16_f32 v29, v10, v11\n"
"v_cvt_pk_bf16_f32 v30, v12, v13\n"
"v_cvt_pk_bf16_f32 v31, v14, v15\n"
"v_mfma_f32_32x32x16_bf16 v[0:15], v[16:19], v[24:27], v[72:87]\n"
"s_waitcnt lgkmcnt(0)\n"
"s_barrier\n"
"v_mfma_f32_32x32x16_bf16 v[72:87], v[20:23], v[28:31], v[72:87]\n"
"ds_read_b128 v[64:67], v117 offset:32768\n"
"ds_read_b128 v[68:71], v117 offset:33792\n"
"ds_read_b128 v[52:55], v113 offset:6144\n"
"v_mfma_f32_32x32x16_bf16 v[32:47], v[144:147], v[56:59], v[32:47]\n"
"ds_read_b32 v60, v122\n"
"ds_read_b32 v61, v123\n"
"ds_read_b32 v62, v124\n"
"ds_read_b32 v63, v125\n"
"ds_read_b128 v[56:59], v113 offset:7168\n"
"v_mfma_f32_32x32x16_bf16 v[32:47], v[148:151], v[48:51], v[32:47]\n"
"ds_read_b128 v[48:51], v113 offset:8192\n"
"s_waitcnt lgkmcnt(6)\n"
"v_mfma_f32_32x32x16_bf16 v[32:47], v[152:155], v[52:55], v[32:47]\n"
"ds_read_b128 v[52:55], v113 offset:9216\n"
"v_mfma_f32_32x32x16_bf16 v[128:143], v[64:67], v[24:27], v[76:91]\n"
"s_waitcnt lgkmcnt(2)\n"
"v_mfma_f32_32x32x16_bf16 v[32:47], v[156:159], v[56:59], v[32:47]\n"
"ds_read_b128 v[56:59], v113 offset:10240\n"
"v_mul_f32 v16, v60, v93\n"
"v_mul_f32 v17, v61, v93\n"
"v_mul_f32 v18, v62, v93\n"
"v_mul_f32 v19, v63, v93\n"
"v_mfma_f32_32x32x16_bf16 v[144:159], v[68:71], v[28:31], v[76:91]\n"
"v_max3_f32 v20, v0, v4, v8\n"
"v_max3_f32 v21, v1, v5, v9\n"
"v_max3_f32 v22, v2, v6, v10\n"
"v_max3_f32 v23, v3, v7, v11\n"
"s_waitcnt lgkmcnt(2)\n"
"v_mfma_f32_32x32x16_bf16 v[32:47], v[160:163], v[48:51], v[32:47]\n"
"ds_read_b128 v[48:51], v113 offset:11264\n"
"v_max3_f32 v20, v20, v12, v140\n"
"v_max3_f32 v21, v21, v13, v141\n"
"v_max3_f32 v22, v22, v14, v142\n"
"v_max3_f32 v23, v23, v15, v143\n"
"v_mul_f32 v20, v20, v16\n"
"s_waitcnt lgkmcnt(2)\n"
"v_mfma_f32_32x32x16_bf16 v[32:47], v[164:167], v[52:55], v[32:47]\n"
"ds_read_b128 v[52:55], v113 offset:12288\n"
"v_mul_f32 v21, v21, v17\n"
"v_mul_f32 v22, v22, v18\n"
"v_mul_f32 v23, v23, v19\n"
"v_max3_f32 v68, v20, v21, v22\n"
"s_waitcnt lgkmcnt(2)\n"
"v_mfma_f32_32x32x16_bf16 v[32:47], v[168:171], v[56:59], v[32:47]\n"
"ds_read_b128 v[56:59], v113 offset:13312\n"
"v_max_f32 v68, v68, v23\n"
"v_max3_f32 v64, v72, v76, v80\n"
"v_max3_f32 v65, v73, v77, v81\n"
"v_max3_f32 v66, v74, v78, v82\n"
"s_waitcnt lgkmcnt(2)\n"
"v_mfma_f32_32x32x16_bf16 v[32:47], v[172:175], v[48:51], v[32:47]\n"
"ds_read_b128 v[48:51], v113 offset:14336\n"
"ds_read_b128 v[160:163], v116 offset:58368\n"
"ds_read_b128 v[164:167], v116 offset:59392\n"
"ds_read_b128 v[168:171], v116 offset:60416\n"
"v_max3_f32 v67, v75, v79, v83\n"
"v_max3_f32 v64, v64, v84, v156\n"
"v_max3_f32 v65, v65, v85, v157\n"
"v_max3_f32 v66, v66, v86, v158\n"
"s_waitcnt lgkmcnt(5)\n"
"v_mfma_f32_32x32x16_bf16 v[32:47], v[176:179], v[52:55], v[32:47]\n"
"ds_read_b128 v[52:55], v113 offset:15360\n"
"v_max3_f32 v67, v67, v87, v159\n"
"v_mul_f32 v64, v64, v16\n"
"v_mul_f32 v65, v65, v17\n"
"v_mul_f32 v66, v66, v18\n"
"s_waitcnt lgkmcnt(5)\n"
"v_mfma_f32_32x32x16_bf16 v[32:47], v[180:183], v[56:59], v[32:47]\n"
"ds_read_b128 v[56:59], v113 offset:16384\n"
"v_mul_f32 v67, v67, v19\n"
"v_max3_f32 v69, v64, v65, v66\n"
"v_max_f32 v69, v69, v67\n"
"s_nop 1\n"
"v_permlane32_swap_b32 v68, v69\n"
"s_waitcnt lgkmcnt(5)\n"
"v_mfma_f32_32x32x16_bf16 v[32:47], v[184:187], v[48:51], v[32:47]\n"
"ds_read_b128 v[48:51], v113 offset:17408\n"
"v_max_f32 v68, v68, v69\n"
"v_mov_b32 v69, v68\n"
"s_nop 1\n"
"v_permlane32_swap_b32 v68, v69\n"
"v_sub_f32 v20, v20, v68\n"
"s_waitcnt lgkmcnt(2)\n"
"v_mfma_f32_32x32x16_bf16 v[32:47], v[188:191], v[52:55], v[32:47]\n"
"ds_read_b128 v[52:55], v113 offset:18432\n"
"v_sub_f32 v21, v21, v68\n"
"v_sub_f32 v22, v22, v68\n"
"v_sub_f32 v23, v23, v68\n"
"v_sub_f32 v64, v64, v69\n"
"s_waitcnt lgkmcnt(2)\n"
"v_mfma_f32_32x32x16_bf16 v[32:47], v[192:195], v[56:59], v[32:47]\n"
"ds_read_b128 v[56:59], v113 offset:19456\n"
"v_sub_f32 v65, v65, v69\n"
"v_sub_f32 v66, v66, v69\n"
"v_sub_f32 v67, v67, v69\n"
"v_exp_f32 v20, v20\n"
"v_exp_f32 v21, v21\n"
"s_waitcnt lgkmcnt(2)\n"
"v_mfma_f32_32x32x16_bf16 v[32:47], v[196:199], v[48:51], v[32:47]\n"
"ds_read_b128 v[48:51], v113 offset:20480\n"
"v_exp_f32 v22, v22\n"
"v_exp_f32 v23, v23\n"
"v_exp_f32 v64, v64\n"
"v_exp_f32 v65, v65\n"
"s_waitcnt lgkmcnt(2)\n"
"v_mfma_f32_32x32x16_bf16 v[32:47], v[200:203], v[52:55], v[32:47]\n"
"ds_read_b128 v[52:55], v113 offset:21504\n"
"ds_read_b128 v[196:199], v94 offset:1024\n"
"v_exp_f32 v66, v66\n"
"v_exp_f32 v67, v67\n"
"v_add_f32 v68, v20, v21\n"
"v_add_f32 v70, v22, v23\n"
"s_waitcnt lgkmcnt(3)\n"
"v_mfma_f32_32x32x16_bf16 v[32:47], v[204:207], v[56:59], v[32:47]\n"
"ds_read_b128 v[56:59], v113 offset:22528\n"
"v_add_f32 v69, v64, v65\n"
"v_add_f32 v71, v66, v67\n"
"v_add_f32 v68, v68, v70\n"
"v_add_f32 v69, v69, v71\n"
"s_waitcnt lgkmcnt(3)\n"
"v_mfma_f32_32x32x16_bf16 v[32:47], v[208:211], v[48:51], v[32:47]\n"
"ds_read_b128 v[48:51], v113 offset:23552\n"
"v_mov_b32 v180, 0\n"
"v_mov_b32 v181, 0\n"
"v_mov_b32 v192, 0\n"
"v_mov_b32 v193, 0\n"
"s_waitcnt lgkmcnt(3)\n"
"v_mfma_f32_32x32x16_bf16 v[32:47], v[212:215], v[52:55], v[32:47]\n"
"ds_read_b128 v[52:55], v113 offset:24576\n"
"v_mul_f32 v20, v20, v60\n"
"v_mul_f32 v21, v21, v61\n"
"v_permlane32_swap_b32 v68, v69\n"
"v_add_f32 v68, v68, v69\n"
"s_waitcnt lgkmcnt(2)\n"
"v_mfma_f32_32x32x16_bf16 v[32:47], v[216:219], v[56:59], v[32:47]\n"
"ds_read_b128 v[56:59], v113 offset:25600\n"
"v_rcp_f32 v68, v68\n"
"v_mul_f32 v22, v22, v62\n"
"v_mov_b32 v69, v68\n"
"v_mul_f32 v23, v23, v63\n"
"s_waitcnt lgkmcnt(2)\n"
"v_mfma_f32_32x32x16_bf16 v[32:47], v[220:223], v[48:51], v[32:47]\n"
"ds_read_b128 v[48:51], v113 offset:26624\n"
"v_mul_f32 v64, v64, v60\n"
"v_permlane32_swap_b32 v68, v69\n"
"v_mul_f32 v65, v65, v61\n"
"v_mul_f32 v66, v66, v62\n"
"s_waitcnt lgkmcnt(2)\n"
"v_mfma_f32_32x32x16_bf16 v[32:47], v[224:227], v[52:55], v[32:47]\n"
"ds_read_b128 v[52:55], v113 offset:27648\n"
"v_mul_f32 v67, v67, v63\n"
"v_mul_f32 v20, v20, v68\n"
"v_mul_f32 v21, v21, v68\n"
"v_mul_f32 v22, v22, v68\n"
"v_mul_f32 v23, v23, v68\n"
"s_waitcnt lgkmcnt(2)\n"
"v_mfma_f32_32x32x16_bf16 v[32:47], v[228:231], v[56:59], v[32:47]\n"
"ds_read_b128 v[56:59], v113 offset:28672\n"
"v_cvt_pk_bf16_f32 v20, v20, v21\n"
"v_cvt_pk_bf16_f32 v21, v22, v23\n"
"v_and_b32 v172, v20, v162\n"
"v_and_b32 v173, v21, v163\n"
"s_waitcnt lgkmcnt(2)\n"
"v_mfma_f32_32x32x16_bf16 v[32:47], v[232:235], v[48:51], v[32:47]\n"
"ds_read_b128 v[48:51], v113 offset:29696\n"
"v_and_b32 v174, v20, v164\n"
"v_and_b32 v175, v21, v165\n"
"v_and_b32 v176, v20, v166\n"
"v_and_b32 v177, v21, v167\n"
"s_waitcnt lgkmcnt(2)\n"
"v_mfma_f32_32x32x16_bf16 v[32:47], v[236:239], v[52:55], v[32:47]\n"
"ds_read_b128 v[52:55], v113 offset:30720\n"
"v_and_b32 v178, v20, v168\n"
"v_and_b32 v179, v21, v169\n"
"v_and_b32 v182, v20, v160\n"
"v_and_b32 v183, v21, v161\n"
"s_waitcnt lgkmcnt(2)\n"
"v_mfma_f32_32x32x16_bf16 v[32:47], v[240:243], v[56:59], v[32:47]\n"
"ds_read_b128 v[56:59], v113 offset:31744\n"
"v_mul_f32 v64, v64, v69\n"
"v_mul_f32 v65, v65, v69\n"
"v_mul_f32 v66, v66, v69\n"
"v_mul_f32 v67, v67, v69\n"
"s_waitcnt lgkmcnt(2)\n"
"v_mfma_f32_32x32x16_bf16 v[32:47], v[244:247], v[48:51], v[32:47]\n"
"v_cvt_pk_bf16_f32 v64, v64, v65\n"
"v_cvt_pk_bf16_f32 v65, v66, v67\n"
"v_and_b32 v184, v64, v162\n"
"v_and_b32 v185, v65, v163\n"
"s_waitcnt lgkmcnt(1)\n"
"v_mfma_f32_32x32x16_bf16 v[32:47], v[248:251], v[52:55], v[32:47]\n"
"v_and_b32 v186, v64, v164\n"
"v_and_b32 v187, v65, v165\n"
"v_and_b32 v188, v64, v166\n"
"v_and_b32 v189, v65, v167\n"
"s_waitcnt lgkmcnt(0)\n"
"v_mfma_f32_32x32x16_bf16 v[32:47], v[252:255], v[56:59], v[32:47]\n"
"v_and_b32 v190, v64, v168\n"
"v_and_b32 v191, v65, v169\n"
"v_and_b32 v194, v64, v160\n"
"v_and_b32 v195, v65, v161\n"
"v_mfma_f32_32x32x16_bf16 v[32:47], v[96:99], v[196:199], v[32:47]\n"
"s_nop 11\n"
"v_cvt_pk_bf16_f32 v40, v40, v41\n"
"v_cvt_pk_bf16_f32 v41, v42, v43\n"
"v_cvt_pk_bf16_f32 v42, v44, v45\n"
"v_cvt_pk_bf16_f32 v43, v46, v47\n"
"ds_write_b128 v117, v[40:43] offset:37888\n"
"v_cvt_pk_bf16_f32 v32, v32, v33\n"
"v_mfma_f32_32x32x16_bf16 v[0:15], v[40:43], v[176:179], 0\n"
"v_cvt_pk_bf16_f32 v33, v34, v35\n"
"v_cvt_pk_bf16_f32 v34, v36, v37\n"
"v_cvt_pk_bf16_f32 v35, v38, v39\n"
"v_mfma_f32_32x32x16_bf16 v[16:31], v[40:43], v[188:191], 0\n"
"s_waitcnt lgkmcnt(0)\n"
"s_barrier\n"
"ds_read_b128 v[44:47], v117 offset:34816\n"
"v_mfma_f32_32x32x16_bf16 v[0:15], v[32:35], v[172:175], v[0:15]\n"
"v_mfma_f32_32x32x16_bf16 v[16:31], v[32:35], v[184:187], v[16:31]\n"
"s_waitcnt lgkmcnt(0)\n"
"v_mfma_f32_32x32x16_bf16 v[0:15], v[44:47], v[180:183], v[0:15]\n"
"v_mfma_f32_32x32x16_bf16 v[16:31], v[44:47], v[192:195], v[16:31]\n"
"s_nop 10\n"
"v_cvt_pk_bf16_f32 v48, v0, v1\n"
"v_cvt_pk_bf16_f32 v49, v2, v3\n"
"v_cvt_pk_bf16_f32 v50, v4, v5\n"
"v_cvt_pk_bf16_f32 v51, v6, v7\n"
"v_min_u32 v48, -2, v48\n"
"buffer_store_dwordx4 v[48:51], v115, s[8:11], s44 offen sc1\n"
"v_cvt_pk_bf16_f32 v52, v24, v25\n"
"v_cvt_pk_bf16_f32 v53, v26, v27\n"
"v_cvt_pk_bf16_f32 v54, v28, v29\n"
"v_cvt_pk_bf16_f32 v55, v30, v31\n"
"v_min_u32 v52, -2, v52\n"
"buffer_store_dwordx4 v[52:55], v115, s[8:11], s44 offen offset:1024 sc1\n"
"v_lshlrev_b32 v32, 16, v104\n"
"v_and_b32 v33, 0xffff0000, v104\n"
"v_lshlrev_b32 v34, 16, v105\n"
"v_and_b32 v35, 0xffff0000, v105\n"
"v_lshlrev_b32 v36, 16, v106\n"
"v_and_b32 v37, 0xffff0000, v106\n"
"v_lshlrev_b32 v38, 16, v107\n"
"v_and_b32 v39, 0xffff0000, v107\n"
"v_lshlrev_b32 v40, 16, v108\n"
"v_and_b32 v41, 0xffff0000, v108\n"
"v_lshlrev_b32 v42, 16, v109\n"
"v_and_b32 v43, 0xffff0000, v109\n"
"v_lshlrev_b32 v44, 16, v110\n"
"v_and_b32 v45, 0xffff0000, v110\n"
"v_lshlrev_b32 v46, 16, v111\n"
"v_and_b32 v47, 0xffff0000, v111\n"
"v_lshrrev_b32 v56, 2, v115\n"
"v_and_b32 v56, 0x30, v56\n"
"v_lshl_add_u32 v56, v115, 2, v56\n"
"v_add_u32 v56, s51, v56\n"
"v_bfe_u32 v3, v115, 4, 5\n"
"v_lshrrev_b32 v2, 9, v115\n"
"v_mul_u32_u24 v0, 272, v3\n"
"v_lshl_add_u32 v0, v2, 4, v0\n"
"s_lshl_b32 s34, s32, 6\n"
"v_add_u32 v0, s34, v0\n"
"s_lshl_b32 s35, s32, 3\n"
"v_lshrrev_b32 v3, 8, v115\n"
"v_add_u32 v3, s35, v3\n"
"v_and_b32 v2, 0xf0, v115\n"
"v_mul_u32_u24 v1, 272, v3\n"
"v_add_u32 v1, v1, v2\n"
"v_lshl_add_u32 v2, v3, 11, v2\n"
"v_subrev_u32 v2, s34, v2\n"
"v_add_u32 v3, 0x2000, v2\n"
"s_waitcnt vmcnt(0)\n"
"s_sleep 5\n"
"s_mov_b32 m0, s51\n"
"s_nop 0\n"
"buffer_load_dwordx4 v115, s[8:11], s45 offen offset:0 sc1 lds\n"
"buffer_load_dwordx4 v115, s[8:11], s45 offen offset:1024 sc1 lds\n"
"buffer_load_dwordx4 v115, s[8:11], s45 offen offset:2048 sc1 lds\n"
"buffer_load_dwordx4 v115, s[8:11], s45 offen offset:3072 sc1 lds\n"
"s_add_u32 m0, s51, 16384\n"
"s_add_u32 s35, s45, 16384\n"
"buffer_load_dwordx4 v115, s[8:11], s35 offen offset:0 sc1 lds\n"
"buffer_load_dwordx4 v115, s[8:11], s35 offen offset:1024 sc1 lds\n"
"buffer_load_dwordx4 v115, s[8:11], s35 offen offset:2048 sc1 lds\n"
"buffer_load_dwordx4 v115, s[8:11], s35 offen offset:3072 sc1 lds\n"
"s_waitcnt vmcnt(4)\n"
"ds_read_b32 v61, v56 offset:0\n"
"s_waitcnt lgkmcnt(0)\n"
"v_cmp_eq_u32 vcc, -1, v61\n"
"s_cbranch_vccnz L_os3\n"
"L_or3:\n"
"s_waitcnt lgkmcnt(0)\n"
"s_barrier\n"
"ds_read_b128 v[128:131], v116 offset:0\n"
"ds_read_b128 v[132:135], v116 offset:1024\n"
"ds_read_b128 v[136:139], v116 offset:2048\n"
"ds_read_b128 v[140:143], v116 offset:3072\n"
"v_mov_b32 v92, 0\n"
"s_waitcnt lgkmcnt(3)\n"
"v_mfma_f32_32x32x16_bf16 v[32:47], a[128:131], v[128:131], v[32:47]\n"
"ds_read_b128 v[144:147], v116 offset:4096\n"
"s_waitcnt lgkmcnt(3)\n"
"v_mfma_f32_32x32x16_bf16 v[32:47], a[132:135], v[132:135], v[32:47]\n"
"ds_read_b128 v[148:151], v116 offset:5120\n"
"v_max3_u32 v92, v92, v128, v132\n"
"s_waitcnt lgkmcnt(3)\n"
"v_mfma_f32_32x32x16_bf16 v[32:47], a[136:139], v[136:139], v[32:47]\n"
"ds_read_b128 v[152:155], v116 offset:6144\n"
"s_waitcnt lgkmcnt(3)\n"
"v_mfma_f32_32x32x16_bf16 v[32:47], a[140:143], v[140:143], v[32:47]\n"
"ds_read_b128 v[156:159], v116 offset:7168\n"
"v_max3_u32 v92, v92, v136, v140\n"
"s_waitcnt lgkmcnt(3)\n"
"v_mfma_f32_32x32x16_bf16 v[32:47], a[144:147], v[144:147], v[32:47]\n"
"ds_read_b128 v[160:163], v116 offset:8192\n"
"s_waitcnt lgkmcnt(3)\n"
"v_mfma_f32_32x32x16_bf16 v[32:47], a[148:151], v[148:151], v[32:47]\n"
"ds_read_b128 v[164:167], v116 offset:9216\n"
"v_max3_u32 v92, v92, v144, v148\n"
"s_waitcnt lgkmcnt(3)\n"
"v_mfma_f32_32x32x16_bf16 v[32:47], a[152:155], v[152:155], v[32:47]\n"
"ds_read_b128 v[168:171], v116 offset:10240\n"
"s_waitcnt lgkmcnt(3)\n"
"v_mfma_f32_32x32x16_bf16 v[32:47], a[156:159], v[156:159], v[32:47]\n"
"ds_read_b128 v[172:175], v116 offset:11264\n"
"v_max3_u32 v92, v92, v152, v156\n"
"s_waitcnt lgkmcnt(3)\n"
"v_mfma_f32_32x32x16_bf16 v[32:47], a[160:163], v[160:163], v[32:47]\n"
"ds_read_b128 v[176:179], v116 offset:12288\n"
"s_waitcnt lgkmcnt(3)\n"
"v_mfma_f32_32x32x16_bf16 v[32:47], a[164:167], v[164:167], v[32:47]\n"
"s_waitcnt vmcnt(0)\n"
"ds_read_b32 v61, v56 offset:16384\n"
"ds_read_b128 v[180:183], v116 offset:13312\n"
"v_max3_u32 v92, v92, v160, v164\n"
"s_waitcnt lgkmcnt(4)\n"
"v_mfma_f32_32x32x16_bf16 v[32:47], a[168:171], v[168:171], v[32:47]\n"
"ds_read_b128 v[184:187], v116 offset:14336\n"
"s_waitcnt lgkmcnt(4)\n"
"v_mfma_f32_32x32x16_bf16 v[32:47], a[172:175], v[172:175], v[32:47]\n"
"s_waitcnt lgkmcnt(2)\n"
"v_cmp_eq_u32 vcc, -1, v61\n"
"s_cbranch_vccnz L_ot3\n"
"L_ou3:\n"
"s_barrier\n"
"ds_read_b128 v[188:191], v116 offset:15360\n"
"v_max3_u32 v92, v92, v168, v172\n"
"v_mfma_f32_32x32x16_bf16 v[32:47], a[176:179], v[176:179], v[32:47]\n"
"ds_read_b128 v[192:195], v116 offset:16384\n"
"s_waitcnt lgkmcnt(3)\n"
"v_mfma_f32_32x32x16_bf16 v[32:47], a[180:183], v[180:183], v[32:47]\n"
"ds_read_b128 v[196:199], v116 offset:17408\n"
"v_max3_u32 v92, v92, v176, v180\n"
"s_waitcnt lgkmcnt(3)\n"
"v_mfma_f32_32x32x16_bf16 v[32:47], a[184:187], v[184:187], v[32:47]\n"
"ds_read_b128 v[200:203], v116 offset:18432\n"
"s_waitcnt lgkmcnt(3)\n"
"v_mfma_f32_32x32x16_bf16 v[32:47], a[188:191], v[188:191], v[32:47]\n"
"ds_read_b128 v[204:207], v116 offset:19456\n"
"v_max3_u32 v92, v92, v184, v188\n"
"s_waitcnt lgkmcnt(3)\n"
"v_mfma_f32_32x32x16_bf16 v[32:47], a[192:195], v[192:195], v[32:47]\n"
"ds_read_b128 v[208:211], v116 offset:20480\n"
"s_waitcnt lgkmcnt(3)\n"
"v_mfma_f32_32x32x16_bf16 v[32:47], a[196:199], v[196:199], v[32:47]\n"
"ds_read_b128 v[212:215], v116 offset:21504\n"
"v_max3_u32 v92, v92, v192, v196\n"
"s_waitcnt lgkmcnt(3)\n"
"v_mfma_f32_32x32x16_bf16 v[32:47], a[200:203], v[200:203], v[32:47]\n"
"ds_read_b128 v[216:219], v116 offset:22528\n"
"s_waitcnt lgkmcnt(3)\n"
"v_mfma_f32_32x32x16_bf16 v[32:47], a[204:207], v[204:207], v[32:47]\n"
"ds_read_b128 v[220:223], v116 offset:23552\n"
"v_max3_u32 v92, v92, v200, v204\n"
"s_waitcnt lgkmcnt(3)\n"
"v_mfma_f32_32x32x16_bf16 v[32:47], a[208:211], v[208:211], v[32:47]\n"
"ds_read_b128 v[224:227], v116 offset:24576\n"
"s_waitcnt lgkmcnt(3)\n"
"v_mfma_f32_32x32x16_bf16 v[32:47], a[212:215], v[212:215], v[32:47]\n"
"ds_read_b128 v[228:231], v116 offset:25600\n"
"v_max3_u32 v92, v92, v208, v212\n"
"s_waitcnt lgkmcnt(3)\n"
"v_mfma_f32_32x32x16_bf16 v[32:47], a[216:219], v[216:219], v[32:47]\n"
"ds_read_b128 v[232:235], v116 offset:26624\n"
"s_waitcnt lgkmcnt(3)\n"
"v_mfma_f32_32x32x16_bf16 v[32:47], a[220:223], v[220:223], v[32:47]\n"
"ds_read_b128 v[236:239], v116 offset:27648\n"
"v_max3_u32 v92, v92, v216, v220\n"
"s_waitcnt lgkmcnt(3)\n"
"v_mfma_f32_32x32x16_bf16 v[32:47], a[224:227], v[224:227], v[32:47]\n"
"ds_read_b128 v[240:243], v116 offset:28672\n"
"s_waitcnt lgkmcnt(3)\n"
"v_mfma_f32_32x32x16_bf16 v[32:47], a[228:231], v[228:231], v[32:47]\n"
"ds_read_b128 v[244:247], v116 offset:29696\n"
"v_max3_u32 v92, v92, v224, v228\n"
"s_waitcnt lgkmcnt(3)\n"
"v_mfma_f32_32x32x16_bf16 v[32:47], a[232:235], v[232:235], v[32:47]\n"
"ds_read_b128 v[248:251], v116 offset:30720\n"
"s_waitcnt lgkmcnt(3)\n"
"v_mfma_f32_32x32x16_bf16 v[32:47], a[236:239], v[236:239], v[32:47]\n"
"ds_read_b128 v[252:255], v116 offset:31744\n"
"v_max3_u32 v92, v92, v232, v236\n"
"s_waitcnt lgkmcnt(3)\n"
"v_mfma_f32_32x32x16_bf16 v[32:47], a[240:243], v[240:243], v[32:47]\n"
"s_waitcnt lgkmcnt(2)\n"
"v_mfma_f32_32x32x16_bf16 v[32:47], a[244:247], v[244:247], v[32:47]\n"
"v_max3_u32 v92, v92, v240, v244\n"
"s_waitcnt lgkmcnt(1)\n"
"v_mfma_f32_32x32x16_bf16 v[32:47], a[248:251], v[248:251], v[32:47]\n"
"s_waitcnt lgkmcnt(0)\n"
"v_mfma_f32_32x32x16_bf16 v[32:47], a[252:255], v[252:255], v[32:47]\n"
"v_max3_u32 v92, v92, v248, v252\n"
"v_cmp_eq_u32 vcc, -1, v92\n"
"s_cbranch_vccnz L_fail_o3\n"
"L_ok_o3:\n"
"s_nop 8\n"
"v_cvt_pk_bf16_f32 v104, v32, v33\n"
"v_cvt_pk_bf16_f32 v105, v34, v35\n"
"v_cvt_pk_bf16_f32 v106, v36, v37\n"
"v_cvt_pk_bf16_f32 v107, v38, v39\n"
"v_cvt_pk_bf16_f32 v108, v40, v41\n"
"v_cvt_pk_bf16_f32 v109, v42, v43\n"
"v_cvt_pk_bf16_f32 v110, v44, v45\n"
"v_cvt_pk_bf16_f32 v111, v46, v47\n"
"v_permlane32_swap_b32 v104, v106\n"
"v_permlane32_swap_b32 v105, v107\n"
"ds_write_b128 v0, v[104:107] offset:0\n"
"v_permlane32_swap_b32 v108, v110\n"
"v_permlane32_swap_b32 v109, v111\n"
"ds_write_b128 v0, v[108:111] offset:32\n"
"s_waitcnt lgkmcnt(0)\n"
"s_barrier\n"
"ds_read_b128 v[4:7], v1 offset:0\n"
"ds_read_b128 v[8:11], v1 offset:1088\n"
"s_waitcnt lgkmcnt(1)\n"
"buffer_store_dwordx4 v[4:7], v2, s[28:31], s62 offen sc1\n"
"s_waitcnt lgkmcnt(0)\n"
"buffer_store_dwordx4 v[8:11], v3, s[28:31], s62 offen sc1\n"
"s_branch L_end\n"
"L_xs0:\n"
"s_add_u32 s34, s40, 0\n"
"buffer_load_dwordx4 v[128:131], v115, s[8:11], s34 offen offset:0 sc1\n"
"buffer_load_dwordx4 v[132:135], v115, s[8:11], s34 offen offset:1024 sc1\n"
"buffer_load_dwordx4 v[136:139], v115, s[8:11], s34 offen offset:2048 sc1\n"
"buffer_load_dwordx4 v[140:143], v115, s[8:11], s34 offen offset:3072 sc1\n"
"s_add_u32 s34, s40, 4096\n"
"buffer_load_dwordx4 v[144:147], v115, s[8:11], s34 offen offset:0 sc1\n"
"buffer_load_dwordx4 v[148:151], v115, s[8:11], s34 offen offset:1024 sc1\n"
"buffer_load_dwordx4 v[152:155], v115, s[8:11], s34 offen offset:2048 sc1\n"
"buffer_load_dwordx4 v[156:159], v115, s[8:11], s34 offen offset:3072 sc1\n"
"s_add_u32 s34, s40, 8192\n"
"buffer_load_dwordx4 v[160:163], v115, s[8:11], s34 offen offset:0 sc1\n"
"buffer_load_dwordx4 v[164:167], v115, s[8:11], s34 offen offset:1024 sc1\n"
"buffer_load_dwordx4 v[168:171], v115, s[8:11], s34 offen offset:2048 sc1\n"
"buffer_load_dwordx4 v[172:175], v115, s[8:11], s34 offen offset:3072 sc1\n"
"s_waitcnt vmcnt(11)\n"
"v_cmp_eq_u32 vcc, -1, v128\n"
"s_cbranch_vccnz L_xs0\n"
"s_branch L_xr0\n"
"L_xs1:\n"
"s_add_u32 s34, s40, 0\n"
"buffer_load_dwordx4 v[132:135], v115, s[8:11], s34 offen offset:1024 sc1\n"
"buffer_load_dwordx4 v[136:139], v115, s[8:11], s34 offen offset:2048 sc1\n"
"buffer_load_dwordx4 v[140:143], v115, s[8:11], s34 offen offset:3072 sc1\n"
"s_add_u32 s34, s40, 4096\n"
"buffer_load_dwordx4 v[144:147], v115, s[8:11], s34 offen offset:0 sc1\n"
"buffer_load_dwordx4 v[148:151], v115, s[8:11], s34 offen offset:1024 sc1\n"
"buffer_load_dwordx4 v[152:155], v115, s[8:11], s34 offen offset:2048 sc1\n"
"buffer_load_dwordx4 v[156:159], v115, s[8:11], s34 offen offset:3072 sc1\n"
"s_add_u32 s34, s40, 8192\n"
"buffer_load_dwordx4 v[160:163], v115, s[8:11], s34 offen offset:0 sc1\n"
"buffer_load_dwordx4 v[164:167], v115, s[8:11], s34 offen offset:1024 sc1\n"
"buffer_load_dwordx4 v[168:171], v115, s[8:11], s34 offen offset:2048 sc1\n"
"buffer_load_dwordx4 v[172:175], v115, s[8:11], s34 offen offset:3072 sc1\n"
"s_add_u32 s34, s40, 12288\n"
"buffer_load_dwordx4 v[176:179], v115, s[8:11], s34 offen offset:0 sc1\n"
"s_waitcnt vmcnt(11)\n"
"v_cmp_eq_u32 vcc, -1, v132\n"
"s_cbranch_vccnz L_xs1\n"
"s_branch L_xr1\n"
"L_xs2:\n"
"s_add_u32 s34, s40, 0\n"
"buffer_load_dwordx4 v[136:139], v115, s[8:11], s34 offen offset:2048 sc1\n"
"buffer_load_dwordx4 v[140:143], v115, s[8:11], s34 offen offset:3072 sc1\n"
"s_add_u32 s34, s40, 4096\n"
"buffer_load_dwordx4 v[144:147], v115, s[8:11], s34 offen offset:0 sc1\n"
"buffer_load_dwordx4 v[148:151], v115, s[8:11], s34 offen offset:1024 sc1\n"
"buffer_load_dwordx4 v[152:155], v115, s[8:11], s34 offen offset:2048 sc1\n"
"buffer_load_dwordx4 v[156:159], v115, s[8:11], s34 offen offset:3072 sc1\n"
"s_add_u32 s34, s40, 8192\n"
"buffer_load_dwordx4 v[160:163], v115, s[8:11], s34 offen offset:0 sc1\n"
"buffer_load_dwordx4 v[164:167], v115, s[8:11], s34 offen offset:1024 sc1\n"
"buffer_load_dwordx4 v[168:171], v115, s[8:11], s34 offen offset:2048 sc1\n"
"buffer_load_dwordx4 v[172:175], v115, s[8:11], s34 offen offset:3072 sc1\n"
"s_add_u32 s34, s40, 12288\n"
"buffer_load_dwordx4 v[176:179], v115, s[8:11], s34 offen offset:0 sc1\n"
"buffer_load_dwordx4 v[180:183], v115, s[8:11], s34 offen offset:1024 sc1\n"
"s_waitcnt vmcnt(11)\n"
"v_cmp_eq_u32 vcc, -1, v136\n"
"s_cbranch_vccnz L_xs2\n"
"s_branch L_xr2\n"
"L_xs3:\n"
"s_add_u32 s34, s40, 0\n"
"buffer_load_dwordx4 v[140:143], v115, s[8:11], s34 offen offset:3072 sc1\n"
"s_add_u32 s34, s40, 4096\n"
"buffer_load_dwordx4 v[144:147], v115, s[8:11], s34 offen offset:0 sc1\n"
"buffer_load_dwordx4 v[148:151], v115, s[8:11], s34 offen offset:1024 sc1\n"
"buffer_load_dwordx4 v[152:155], v115, s[8:11], s34 offen offset:2048 sc1\n"
"buffer_load_dwordx4 v[156:159], v115, s[8:11], s34 offen offset:3072 sc1\n"
"s_add_u32 s34, s40, 8192\n"
"buffer_load_dwordx4 v[160:163], v115, s[8:11], s34 offen offset:0 sc1\n"
"buffer_load_dwordx4 v[164:167], v115, s[8:11], s34 offen offset:1024 sc1\n"
"buffer_load_dwordx4 v[168:171], v115, s[8:11], s34 offen offset:2048 sc1\n"
"buffer_load_dwordx4 v[172:175], v115, s[8:11], s34 offen offset:3072 sc1\n"
"s_add_u32 s34, s40, 12288\n"
"buffer_load_dwordx4 v[176:179], v115, s[8:11], s34 offen offset:0 sc1\n"
"buffer_load_dwordx4 v[180:183], v115, s[8:11], s34 offen offset:1024 sc1\n"
"buffer_load_dword v120, v115, s[8:11], s34 offen sc1\n"
"buffer_load_dword v120, v115, s[8:11], s34 offen sc1\n"
"buffer_load_dwordx4 v[184:187], v115, s[8:11], s34 offen offset:2048 sc1\n"
"s_waitcnt vmcnt(13)\n"
"v_cmp_eq_u32 vcc, -1, v140\n"
"s_cbranch_vccnz L_xs3\n"
"s_branch L_xr3\n"
"L_xs4:\n"
"s_add_u32 s34, s40, 4096\n"
"buffer_load_dwordx4 v[144:147], v115, s[8:11], s34 offen offset:0 sc1\n"
"buffer_load_dwordx4 v[148:151], v115, s[8:11], s34 offen offset:1024 sc1\n"
"buffer_load_dwordx4 v[152:155], v115, s[8:11], s34 offen offset:2048 sc1\n"
"buffer_load_dwordx4 v[156:159], v115, s[8:11], s34 offen offset:3072 sc1\n"
"s_add_u32 s34, s40, 8192\n"
"buffer_load_dwordx4 v[160:163], v115, s[8:11], s34 offen offset:0 sc1\n"
"buffer_load_dwordx4 v[164:167], v115, s[8:11], s34 offen offset:1024 sc1\n"
"buffer_load_dwordx4 v[168:171], v115, s[8:11], s34 offen offset:2048 sc1\n"
"buffer_load_dwordx4 v[172:175], v115, s[8:11], s34 offen offset:3072 sc1\n"
"s_add_u32 s34, s40, 12288\n"
"buffer_load_dwordx4 v[176:179], v115, s[8:11], s34 offen offset:0 sc1\n"
"buffer_load_dwordx4 v[180:183], v115, s[8:11], s34 offen offset:1024 sc1\n"
"buffer_load_dword v120, v115, s[8:11], s34 offen sc1\n"
"buffer_load_dword v120, v115, s[8:11], s34 offen sc1\n"
"buffer_load_dwordx4 v[184:187], v115, s[8:11], s34 offen offset:2048 sc1\n"
"buffer_load_dwordx4 v[188:191], v115, s[8:11], s34 offen offset:3072 sc1\n"
"s_waitcnt vmcnt(13)\n"
"v_cmp_eq_u32 vcc, -1, v144\n"
"s_cbranch_vccnz L_xs4\n"
"s_branch L_xr4\n"
"L_xs5:\n"
"s_add_u32 s34, s40, 4096\n"
"buffer_load_dwordx4 v[148:151], v115, s[8:11], s34 offen offset:1024 sc1\n"
"buffer_load_dwordx4 v[152:155], v115, s[8:11], s34 offen offset:2048 sc1\n"
"buffer_load_dwordx4 v[156:159], v115, s[8:11], s34 offen offset:3072 sc1\n"
"s_add_u32 s34, s40, 8192\n"
"buffer_load_dwordx4 v[160:163], v115, s[8:11], s34 offen offset:0 sc1\n"
"buffer_load_dwordx4 v[164:167], v115, s[8:11], s34 offen offset:1024 sc1\n"
"buffer_load_dwordx4 v[168:171], v115, s[8:11], s34 offen offset:2048 sc1\n"
"buffer_load_dwordx4 v[172:175], v115, s[8:11], s34 offen offset:3072 sc1\n"
"s_add_u32 s34, s40, 12288\n"
"buffer_load_dwordx4 v[176:179], v115, s[8:11], s34 offen offset:0 sc1\n"
"buffer_load_dwordx4 v[180:183], v115, s[8:11], s34 offen offset:1024 sc1\n"
"buffer_load_dword v120, v115, s[8:11], s34 offen sc1\n"
"buffer_load_dword v120, v115, s[8:11], s34 offen sc1\n"
"buffer_load_dwordx4 v[184:187], v115, s[8:11], s34 offen offset:2048 sc1\n"
"buffer_load_dwordx4 v[188:191], v115, s[8:11], s34 offen offset:3072 sc1\n"
"buffer_load_dword v120, v115, s[8:11], s34 offen sc1\n"
"s_add_u32 s34, s40, 16384\n"
"buffer_load_dwordx4 v[192:195], v115, s[8:11], s34 offen offset:0 sc1\n"
"s_waitcnt vmcnt(14)\n"
"v_cmp_eq_u32 vcc, -1, v148\n"
"s_cbranch_vccnz L_xs5\n"
"s_branch L_xr5\n"
"L_xs6:\n"
"s_add_u32 s34, s40, 4096\n"
"buffer_load_dwordx4 v[152:155], v115, s[8:11], s34 offen offset:2048 sc1\n"
"buffer_load_dwordx4 v[156:159], v115, s[8:11], s34 offen offset:3072 sc1\n"
"s_add_u32 s34, s40, 8192\n"
"buffer_load_dwordx4 v[160:163], v115, s[8:11], s34 offen offset:0 sc1\n"
"buffer_load_dwordx4 v[164:167], v115, s[8:11], s34 offen offset:1024 sc1\n"
"buffer_load_dwordx4 v[168:171], v115, s[8:11], s34 offen offset:2048 sc1\n"
"buffer_load_dwordx4 v[172:175], v115, s[8:11], s34 offen offset:3072 sc1\n"
"s_add_u32 s34, s40, 12288\n"
"buffer_load_dwordx4 v[176:179], v115, s[8:11], s34 offen offset:0 sc1\n"
"buffer_load_dwordx4 v[180:183], v115, s[8:11], s34 offen offset:1024 sc1\n"
"buffer_load_dword v120, v115, s[8:11], s34 offen sc1\n"
"buffer_load_dword v120, v115, s[8:11], s34 offen sc1\n"
"buffer_load_dwordx4 v[184:187], v115, s[8:11], s34 offen offset:2048 sc1\n"
"buffer_load_dwordx4 v[188:191], v115, s[8:11], s34 offen offset:3072 sc1\n"
"buffer_load_dword v120, v115, s[8:11], s34 offen sc1\n"
"s_add_u32 s34, s40, 16384\n"
"buffer_load_dwordx4 v[192:195], v115, s[8:11], s34 offen offset:0 sc1\n"
"buffer_load_dword v120, v115, s[8:11], s34 offen sc1\n"
"buffer_load_dwordx4 v[196:199], v115, s[8:11], s34 offen offset:1024 sc1\n"
"s_waitcnt vmcnt(15)\n"
"v_cmp_eq_u32 vcc, -1, v152\n"
"s_cbranch_vccnz L_xs6\n"
"s_branch L_xr6\n"
"L_xs7:\n"
"s_add_u32 s34, s40, 4096\n"
"buffer_load_dwordx4 v[156:159], v115, s[8:11], s34 offen offset:3072 sc1\n"
"s_add_u32 s34, s40, 8192\n"
"buffer_load_dwordx4 v[160:163], v115, s[8:11], s34 offen offset:0 sc1\n"
"buffer_load_dwordx4 v[164:167], v115, s[8:11], s34 offen offset:1024 sc1\n"
"buffer_load_dwordx4 v[168:171], v115, s[8:11], s34 offen offset:2048 sc1\n"
"buffer_load_dwordx4 v[172:175], v115, s[8:11], s34 offen offset:3072 sc1\n"
"s_add_u32 s34, s40, 12288\n"
"buffer_load_dwordx4 v[176:179], v115, s[8:11], s34 offen offset:0 sc1\n"
"buffer_load_dwordx4 v[180:183], v115, s[8:11], s34 offen offset:1024 sc1\n"
"buffer_load_dword v120, v115, s[8:11], s34 offen sc1\n"
"buffer_load_dword v120, v115, s[8:11], s34 offen sc1\n"
"buffer_load_dwordx4 v[184:187], v115, s[8:11], s34 offen offset:2048 sc1\n"
"buffer_load_dwordx4 v[188:191], v115, s[8:11], s34 offen offset:3072 sc1\n"
"buffer_load_dword v120, v115, s[8:11], s34 offen sc1\n"
"s_add_u32 s34, s40, 16384\n"
"buffer_load_dwordx4 v[192:195], v115, s[8:11], s34 offen offset:0 sc1\n"
"buffer_load_dword v120, v115, s[8:11], s34 offen sc1\n"
"buffer_load_dwordx4 v[196:199], v115, s[8:11], s34 offen offset:1024 sc1\n"
"buffer_load_dwordx4 v[200:203], v115, s[8:11], s34 offen offset:2048 sc1\n"
"s_waitcnt vmcnt(15)\n"
"v_cmp_eq_u32 vcc, -1, v156\n"
"s_cbranch_vccnz L_xs7\n"
"s_branch L_xr7\n"
"L_xs8:\n"
"s_add_u32 s34, s40, 8192\n"
"buffer_load_dwordx4 v[160:163], v115, s[8:11], s34 offen offset:0 sc1\n"
"buffer_load_dwordx4 v[164:167], v115, s[8:11], s34 offen offset:1024 sc1\n"
"buffer_load_dwordx4 v[168:171], v115, s[8:11], s34 offen offset:2048 sc1\n"
"buffer_load_dwordx4 v[172:175], v115, s[8:11], s34 offen offset:3072 sc1\n"
"s_add_u32 s34, s40, 12288\n"
"buffer_load_dwordx4 v[176:179], v115, s[8:11], s34 offen offset:0 sc1\n"
"buffer_load_dwordx4 v[180:183], v115, s[8:11], s34 offen offset:1024 sc1\n"
"buffer_load_dword v120, v115, s[8:11], s34 offen sc1\n"
"buffer_load_dword v120, v115, s[8:11], s34 offen sc1\n"
"buffer_load_dwordx4 v[184:187], v115, s[8:11], s34 offen offset:2048 sc1\n"
"buffer_load_dwordx4 v[188:191], v115, s[8:11], s34 offen offset:3072 sc1\n"
"buffer_load_dword v120, v115, s[8:11], s34 offen sc1\n"
"s_add_u32 s34, s40, 16384\n"
"buffer_load_dwordx4 v[192:195], v115, s[8:11], s34 offen offset:0 sc1\n"
"buffer_load_dword v120, v115, s[8:11], s34 offen sc1\n"
"buffer_load_dwordx4 v[196:199], v115, s[8:11], s34 offen offset:1024 sc1\n"
"buffer_load_dwordx4 v[200:203], v115, s[8:11], s34 offen offset:2048 sc1\n"
"buffer_load_dwordx4 v[204:207], v115, s[8:11], s34 offen offset:3072 sc1\n"
"s_waitcnt vmcnt(15)\n"
"v_cmp_eq_u32 vcc, -1, v160\n"
"s_cbranch_vccnz L_xs8\n"
"s_branch L_xr8\n"
"L_xs9:\n"
"s_add_u32 s34, s40, 8192\n"
"buffer_load_dwordx4 v[164:167], v115, s[8:11], s34 offen offset:1024 sc1\n"
"buffer_load_dwordx4 v[168:171], v115, s[8:11], s34 offen offset:2048 sc1\n"
"buffer_load_dwordx4 v[172:175], v115, s[8:11], s34 offen offset:3072 sc1\n"
"s_add_u32 s34, s40, 12288\n"
"buffer_load_dwordx4 v[176:179], v115, s[8:11], s34 offen offset:0 sc1\n"
"buffer_load_dwordx4 v[180:183], v115, s[8:11], s34 offen offset:1024 sc1\n"
"buffer_load_dword v120, v115, s[8:11], s34 offen sc1\n"
"buffer_load_dword v120, v115, s[8:11], s34 offen sc1\n"
"buffer_load_dwordx4 v[184:187], v115, s[8:11], s34 offen offset:2048 sc1\n"
"buffer_load_dwordx4 v[188:191], v115, s[8:11], s34 offen offset:3072 sc1\n"
"buffer_load_dword v120, v115, s[8:11], s34 offen sc1\n"
"s_add_u32 s34, s40, 16384\n"
"buffer_load_dwordx4 v[192:195], v115, s[8:11], s34 offen offset:0 sc1\n"
"buffer_load_dword v120, v115, s[8:11], s34 offen sc1\n"
"buffer_load_dwordx4 v[196:199], v115, s[8:11], s34 offen offset:1024 sc1\n"
"buffer_load_dwordx4 v[200:203], v115, s[8:11], s34 offen offset:2048 sc1\n"
"buffer_load_dwordx4 v[204:207], v115, s[8:11], s34 offen offset:3072 sc1\n"
"s_add_u32 s34, s40, 20480\n"
"buffer_load_dwordx4 v[208:211], v115, s[8:11], s34 offen offset:0 sc1\n"
"s_waitcnt vmcnt(15)\n"
"v_cmp_eq_u32 vcc, -1, v164\n"
"s_cbranch_vccnz L_xs9\n"
"s_branch L_xr9\n"
"L_xs10:\n"
"s_add_u32 s34, s40, 8192\n"
"buffer_load_dwordx4 v[168:171], v115, s[8:11], s34 offen offset:2048 sc1\n"
"buffer_load_dwordx4 v[172:175], v115, s[8:11], s34 offen offset:3072 sc1\n"
"s_add_u32 s34, s40, 12288\n"
"buffer_load_dwordx4 v[176:179], v115, s[8:11], s34 offen offset:0 sc1\n"
"buffer_load_dwordx4 v[180:183], v115, s[8:11], s34 offen offset:1024 sc1\n"
"buffer_load_dword v120, v115, s[8:11], s34 offen sc1\n"
"buffer_load_dword v120, v115, s[8:11], s34 offen sc1\n"
"buffer_load_dwordx4 v[184:187], v115, s[8:11], s34 offen offset:2048 sc1\n"
"buffer_load_dwordx4 v[188:191], v115, s[8:11], s34 offen offset:3072 sc1\n"
"buffer_load_dword v120, v115, s[8:11], s34 offen sc1\n"
"s_add_u32 s34, s40, 16384\n"
"buffer_load_dwordx4 v[192:195], v115, s[8:11], s34 offen offset:0 sc1\n"
"buffer_load_dword v120, v115, s[8:11], s34 offen sc1\n"
"buffer_load_dwordx4 v[196:199], v115, s[8:11], s34 offen offset:1024 sc1\n"
"buffer_load_dwordx4 v[200:203], v115, s[8:11], s34 offen offset:2048 sc1\n"
"buffer_load_dwordx4 v[204:207], v115, s[8:11], s34 offen offset:3072 sc1\n"
"s_add_u32 s34, s40, 20480\n"
"buffer_load_dwordx4 v[208:211], v115, s[8:11], s34 offen offset:0 sc1\n"
"buffer_load_dwordx4 v[212:215], v115, s[8:11], s34 offen offset:1024 sc1\n"
"s_waitcnt vmcnt(15)\n"
"v_cmp_eq_u32 vcc, -1, v168\n"
"s_cbranch_vccnz L_xs10\n"
"s_branch L_xr10\n"
"L_xs11:\n"
"s_add_u32 s34, s40, 8192\n"
"buffer_load_dwordx4 v[172:175], v115, s[8:11], s34 offen offset:3072 sc1\n"
"s_add_u32 s34, s40, 12288\n"
"buffer_load_dwordx4 v[176:179], v115, s[8:11], s34 offen offset:0 sc1\n"
"buffer_load_dwordx4 v[180:183], v115, s[8:11], s34 offen offset:1024 sc1\n"
"buffer_load_dword v120, v115, s[8:11], s34 offen sc1\n"
"buffer_load_dword v120, v115, s[8:11], s34 offen sc1\n"
"buffer_load_dwordx4 v[184:187], v115, s[8:11], s34 offen offset:2048 sc1\n"
"buffer_load_dwordx4 v[188:191], v115, s[8:11], s34 offen offset:3072 sc1\n"
"buffer_load_dword v120, v115, s[8:11], s34 offen sc1\n"
"s_add_u32 s34, s40, 16384\n"
"buffer_load_dwordx4 v[192:195], v115, s[8:11], s34 offen offset:0 sc1\n"
"buffer_load_dword v120, v115, s[8:11], s34 offen sc1\n"
"buffer_load_dwordx4 v[196:199], v115, s[8:11], s34 offen offset:1024 sc1\n"
"buffer_load_dwordx4 v[200:203], v115, s[8:11], s34 offen offset:2048 sc1\n"
"buffer_load_dwordx4 v[204:207], v115, s[8:11], s34 offen offset:3072 sc1\n"
"s_add_u32 s34, s40, 20480\n"
"buffer_load_dwordx4 v[208:211], v115, s[8:11], s34 offen offset:0 sc1\n"
"buffer_load_dwordx4 v[212:215], v115, s[8:11], s34 offen offset:1024 sc1\n"
"buffer_load_dwordx4 v[216:219], v115, s[8:11], s34 offen offset:2048 sc1\n"
"s_waitcnt vmcnt(15)\n"
"v_cmp_eq_u32 vcc, -1, v172\n"
"s_cbranch_vccnz L_xs11\n"
"s_branch L_xr11\n"
"L_xs12:\n"
"s_add_u32 s34, s40, 12288\n"
"buffer_load_dwordx4 v[176:179], v115, s[8:11], s34 offen offset:0 sc1\n"
"buffer_load_dwordx4 v[180:183], v115, s[8:11], s34 offen offset:1024 sc1\n"
"buffer_load_dword v120, v115, s[8:11], s34 offen sc1\n"
"buffer_load_dword v120, v115, s[8:11], s34 offen sc1\n"
"buffer_load_dwordx4 v[184:187], v115, s[8:11], s34 offen offset:2048 sc1\n"
"buffer_load_dwordx4 v[188:191], v115, s[8:11], s34 offen offset:3072 sc1\n"
"buffer_load_dword v120, v115, s[8:11], s34 offen sc1\n"
"s_add_u32 s34, s40, 16384\n"
"buffer_load_dwordx4 v[192:195], v115, s[8:11], s34 offen offset:0 sc1\n"
"buffer_load_dword v120, v115, s[8:11], s34 offen sc1\n"
"buffer_load_dwordx4 v[196:199], v115, s[8:11], s34 offen offset:1024 sc1\n"
"buffer_load_dwordx4 v[200:203], v115, s[8:11], s34 offen offset:2048 sc1\n"
"buffer_load_dwordx4 v[204:207], v115, s[8:11], s34 offen offset:3072 sc1\n"
"s_add_u32 s34, s40, 20480\n"
"buffer_load_dwordx4 v[208:211], v115, s[8:11], s34 offen offset:0 sc1\n"
"buffer_load_dwordx4 v[212:215], v115, s[8:11], s34 offen offset:1024 sc1\n"
"buffer_load_dwordx4 v[216:219], v115, s[8:11], s34 offen offset:2048 sc1\n"
"buffer_load_dwordx4 v[220:223], v115, s[8:11], s34 offen offset:3072 sc1\n"
"s_waitcnt vmcnt(15)\n"
"v_cmp_eq_u32 vcc, -1, v176\n"
"s_cbranch_vccnz L_xs12\n"
"s_branch L_xr12\n"
"L_xs13:\n"
"s_add_u32 s34, s40, 12288\n"
"buffer_load_dwordx4 v[180:183], v115, s[8:11], s34 offen offset:1024 sc1\n"
"buffer_load_dword v120, v115, s[8:11], s34 offen sc1\n"
"buffer_load_dword v120, v115, s[8:11], s34 offen sc1\n"
"buffer_load_dwordx4 v[184:187], v115, s[8:11], s34 offen offset:2048 sc1\n"
"buffer_load_dwordx4 v[188:191], v115, s[8:11], s34 offen offset:3072 sc1\n"
"buffer_load_dword v120, v115, s[8:11], s34 offen sc1\n"
"s_add_u32 s34, s40, 16384\n"
"buffer_load_dwordx4 v[192:195], v115, s[8:11], s34 offen offset:0 sc1\n"
"buffer_load_dword v120, v115, s[8:11], s34 offen sc1\n"
"buffer_load_dwordx4 v[196:199], v115, s[8:11], s34 offen offset:1024 sc1\n"
"buffer_load_dwordx4 v[200:203], v115, s[8:11], s34 offen offset:2048 sc1\n"
"buffer_load_dwordx4 v[204:207], v115, s[8:11], s34 offen offset:3072 sc1\n"
"s_add_u32 s34, s40, 20480\n"
"buffer_load_dwordx4 v[208:211], v115, s[8:11], s34 offen offset:0 sc1\n"
"buffer_load_dwordx4 v[212:215], v115, s[8:11], s34 offen offset:1024 sc1\n"
"buffer_load_dwordx4 v[216:219], v115, s[8:11], s34 offen offset:2048 sc1\n"
"buffer_load_dwordx4 v[220:223], v115, s[8:11], s34 offen offset:3072 sc1\n"
"s_add_u32 s34, s40, 24576\n"
"buffer_load_dwordx4 v[224:227], v115, s[8:11], s34 offen offset:0 sc1\n"
"s_waitcnt vmcnt(15)\n"
"v_cmp_eq_u32 vcc, -1, v180\n"
"s_cbranch_vccnz L_xs13\n"
"s_branch L_xr13\n"
"L_xs14:\n"
"s_add_u32 s34, s40, 12288\n"
"buffer_load_dwordx4 v[184:187], v115, s[8:11], s34 offen offset:2048 sc1\n"
"buffer_load_dwordx4 v[188:191], v115, s[8:11], s34 offen offset:3072 sc1\n"
"buffer_load_dword v120, v115, s[8:11], s34 offen sc1\n"
"s_add_u32 s34, s40, 16384\n"
"buffer_load_dwordx4 v[192:195], v115, s[8:11], s34 offen offset:0 sc1\n"
"buffer_load_dword v120, v115, s[8:11], s34 offen sc1\n"
"buffer_load_dwordx4 v[196:199], v115, s[8:11], s34 offen offset:1024 sc1\n"
"buffer_load_dwordx4 v[200:203], v115, s[8:11], s34 offen offset:2048 sc1\n"
"buffer_load_dwordx4 v[204:207], v115, s[8:11], s34 offen offset:3072 sc1\n"
"s_add_u32 s34, s40, 20480\n"
"buffer_load_dwordx4 v[208:211], v115, s[8:11], s34 offen offset:0 sc1\n"
"buffer_load_dwordx4 v[212:215], v115, s[8:11], s34 offen offset:1024 sc1\n"
"buffer_load_dwordx4 v[216:219], v115, s[8:11], s34 offen offset:2048 sc1\n"
"buffer_load_dwordx4 v[220:223], v115, s[8:11], s34 offen offset:3072 sc1\n"
"s_add_u32 s34, s40, 24576\n"
"buffer_load_dwordx4 v[224:227], v115, s[8:11], s34 offen offset:0 sc1\n"
"buffer_load_dwordx4 v[228:231], v115, s[8:11], s34 offen offset:1024 sc1\n"
"s_waitcnt vmcnt(13)\n"
"v_cmp_eq_u32 vcc, -1, v184\n"
"s_cbranch_vccnz L_xs14\n"
"s_branch L_xr14\n"
"L_xs15:\n"
"s_add_u32 s34, s40, 12288\n"
"buffer_load_dwordx4 v[188:191], v115, s[8:11], s34 offen offset:3072 sc1\n"
"buffer_load_dword v120, v115, s[8:11], s34 offen sc1\n"
"s_add_u32 s34, s40, 16384\n"
"buffer_load_dwordx4 v[192:195], v115, s[8:11], s34 offen offset:0 sc1\n"
"buffer_load_dword v120, v115, s[8:11], s34 offen sc1\n"
"buffer_load_dwordx4 v[196:199], v115, s[8:11], s34 offen offset:1024 sc1\n"
"buffer_load_dwordx4 v[200:203], v115, s[8:11], s34 offen offset:2048 sc1\n"
"buffer_load_dwordx4 v[204:207], v115, s[8:11], s34 offen offset:3072 sc1\n"
"s_add_u32 s34, s40, 20480\n"
"buffer_load_dwordx4 v[208:211], v115, s[8:11], s34 offen offset:0 sc1\n"
"buffer_load_dwordx4 v[212:215], v115, s[8:11], s34 offen offset:1024 sc1\n"
"buffer_load_dwordx4 v[216:219], v115, s[8:11], s34 offen offset:2048 sc1\n"
"buffer_load_dwordx4 v[220:223], v115, s[8:11], s34 offen offset:3072 sc1\n"
"s_add_u32 s34, s40, 24576\n"
"buffer_load_dwordx4 v[224:227], v115, s[8:11], s34 offen offset:0 sc1\n"
"buffer_load_dwordx4 v[228:231], v115, s[8:11], s34 offen offset:1024 sc1\n"
"buffer_load_dwordx4 v[232:235], v115, s[8:11], s34 offen offset:2048 sc1\n"
"s_waitcnt vmcnt(13)\n"
"v_cmp_eq_u32 vcc, -1, v188\n"
"s_cbranch_vccnz L_xs15\n"
"s_branch L_xr15\n"
"L_xs16:\n"
"s_add_u32 s34, s40, 16384\n"
"buffer_load_dwordx4 v[192:195], v115, s[8:11], s34 offen offset:0 sc1\n"
"buffer_load_dword v120, v115, s[8:11], s34 offen sc1\n"
"buffer_load_dwordx4 v[196:199], v115, s[8:11], s34 offen offset:1024 sc1\n"
"buffer_load_dwordx4 v[200:203], v115, s[8:11], s34 offen offset:2048 sc1\n"
"buffer_load_dwordx4 v[204:207], v115, s[8:11], s34 offen offset:3072 sc1\n"
"s_add_u32 s34, s40, 20480\n"
"buffer_load_dwordx4 v[208:211], v115, s[8:11], s34 offen offset:0 sc1\n"
"buffer_load_dwordx4 v[212:215], v115, s[8:11], s34 offen offset:1024 sc1\n"
"buffer_load_dwordx4 v[216:219], v115, s[8:11], s34 offen offset:2048 sc1\n"
"buffer_load_dwordx4 v[220:223], v115, s[8:11], s34 offen offset:3072 sc1\n"
"s_add_u32 s34, s40, 24576\n"
"buffer_load_dwordx4 v[224:227], v115, s[8:11], s34 offen offset:0 sc1\n"
"buffer_load_dwordx4 v[228:231], v115, s[8:11], s34 offen offset:1024 sc1\n"
"buffer_load_dwordx4 v[232:235], v115, s[8:11], s34 offen offset:2048 sc1\n"
"buffer_load_dwordx4 v[236:239], v115, s[8:11], s34 offen offset:3072 sc1\n"
"s_waitcnt vmcnt(12)\n"
"v_cmp_eq_u32 vcc, -1, v192\n"
"s_cbranch_vccnz L_xs16\n"
"s_branch L_xr16\n"
"L_xs17:\n"
"s_add_u32 s34, s40, 16384\n"
"buffer_load_dwordx4 v[196:199], v115, s[8:11], s34 offen offset:1024 sc1\n"
"buffer_load_dwordx4 v[200:203], v115, s[8:11], s34 offen offset:2048 sc1\n"
"buffer_load_dwordx4 v[204:207], v115, s[8:11], s34 offen offset:3072 sc1\n"
"s_add_u32 s34, s40, 20480\n"
"buffer_load_dwordx4 v[208:211], v115, s[8:11], s34 offen offset:0 sc1\n"
"buffer_load_dwordx4 v[212:215], v115, s[8:11], s34 offen offset:1024 sc1\n"
"buffer_load_dwordx4 v[216:219], v115, s[8:11], s34 offen offset:2048 sc1\n"
"buffer_load_dwordx4 v[220:223], v115, s[8:11], s34 offen offset:3072 sc1\n"
"s_add_u32 s34, s40, 24576\n"
"buffer_load_dwordx4 v[224:227], v115, s[8:11], s34 offen offset:0 sc1\n"
"buffer_load_dwordx4 v[228:231], v115, s[8:11], s34 offen offset:1024 sc1\n"
"buffer_load_dwordx4 v[232:235], v115, s[8:11], s34 offen offset:2048 sc1\n"
"buffer_load_dwordx4 v[236:239], v115, s[8:11], s34 offen offset:3072 sc1\n"
"s_add_u32 s34, s40, 28672\n"
"buffer_load_dwordx4 v[240:243], v115, s[8:11], s34 offen offset:0 sc1\n"
"s_waitcnt vmcnt(11)\n"
"v_cmp_eq_u32 vcc, -1, v196\n"
"s_cbranch_vccnz L_xs17\n"
"s_branch L_xr17\n"
"L_xs18:\n"
"s_add_u32 s34, s40, 16384\n"
"buffer_load_dwordx4 v[200:203], v115, s[8:11], s34 offen offset:2048 sc1\n"
"buffer_load_dwordx4 v[204:207], v115, s[8:11], s34 offen offset:3072 sc1\n"
"s_add_u32 s34, s40, 20480\n"
"buffer_load_dwordx4 v[208:211], v115, s[8:11], s34 offen offset:0 sc1\n"
"buffer_load_dwordx4 v[212:215], v115, s[8:11], s34 offen offset:1024 sc1\n"
"buffer_load_dwordx4 v[216:219], v115, s[8:11], s34 offen offset:2048 sc1\n"
"buffer_load_dwordx4 v[220:223], v115, s[8:11], s34 offen offset:3072 sc1\n"
"s_add_u32 s34, s40, 24576\n"
"buffer_load_dwordx4 v[224:227], v115, s[8:11], s34 offen offset:0 sc1\n"
"buffer_load_dwordx4 v[228:231], v115, s[8:11], s34 offen offset:1024 sc1\n"
"buffer_load_dwordx4 v[232:235], v115, s[8:11], s34 offen offset:2048 sc1\n"
"buffer_load_dwordx4 v[236:239], v115, s[8:11], s34 offen offset:3072 sc1\n"
"s_add_u32 s34, s40, 28672\n"
"buffer_load_dwordx4 v[240:243], v115, s[8:11], s34 offen offset:0 sc1\n"
"buffer_load_dwordx4 v[244:247], v115, s[8:11], s34 offen offset:1024 sc1\n"
"s_waitcnt vmcnt(11)\n"
"v_cmp_eq_u32 vcc, -1, v200\n"
"s_cbranch_vccnz L_xs18\n"
"s_branch L_xr18\n"
"L_xs19:\n"
"s_add_u32 s34, s40, 16384\n"
"buffer_load_dwordx4 v[204:207], v115, s[8:11], s34 offen offset:3072 sc1\n"
"s_add_u32 s34, s40, 20480\n"
"buffer_load_dwordx4 v[208:211], v115, s[8:11], s34 offen offset:0 sc1\n"
"buffer_load_dwordx4 v[212:215], v115, s[8:11], s34 offen offset:1024 sc1\n"
"buffer_load_dwordx4 v[216:219], v115, s[8:11], s34 offen offset:2048 sc1\n"
"buffer_load_dwordx4 v[220:223], v115, s[8:11], s34 offen offset:3072 sc1\n"
"s_add_u32 s34, s40, 24576\n"
"buffer_load_dwordx4 v[224:227], v115, s[8:11], s34 offen offset:0 sc1\n"
"buffer_load_dwordx4 v[228:231], v115, s[8:11], s34 offen offset:1024 sc1\n"
"buffer_load_dwordx4 v[232:235], v115, s[8:11], s34 offen offset:2048 sc1\n"
"buffer_load_dwordx4 v[236:239], v115, s[8:11], s34 offen offset:3072 sc1\n"
"s_add_u32 s34, s40, 28672\n"
"buffer_load_dwordx4 v[240:243], v115, s[8:11], s34 offen offset:0 sc1\n"
"buffer_load_dwordx4 v[244:247], v115, s[8:11], s34 offen offset:1024 sc1\n"
"buffer_load_dwordx4 v[248:251], v115, s[8:11], s34 offen offset:2048 sc1\n"
"s_waitcnt vmcnt(11)\n"
"v_cmp_eq_u32 vcc, -1, v204\n"
"s_cbranch_vccnz L_xs19\n"
"s_branch L_xr19\n"
"L_xs20:\n"
"s_add_u32 s34, s40, 20480\n"
"buffer_load_dwordx4 v[208:211], v115, s[8:11], s34 offen offset:0 sc1\n"
"buffer_load_dwordx4 v[212:215], v115, s[8:11], s34 offen offset:1024 sc1\n"
"buffer_load_dwordx4 v[216:219], v115, s[8:11], s34 offen offset:2048 sc1\n"
"buffer_load_dwordx4 v[220:223], v115, s[8:11], s34 offen offset:3072 sc1\n"
"s_add_u32 s34, s40, 24576\n"
"buffer_load_dwordx4 v[224:227], v115, s[8:11], s34 offen offset:0 sc1\n"
"buffer_load_dwordx4 v[228:231], v115, s[8:11], s34 offen offset:1024 sc1\n"
"buffer_load_dwordx4 v[232:235], v115, s[8:11], s34 offen offset:2048 sc1\n"
"buffer_load_dwordx4 v[236:239], v115, s[8:11], s34 offen offset:3072 sc1\n"
"s_add_u32 s34, s40, 28672\n"
"buffer_load_dwordx4 v[240:243], v115, s[8:11], s34 offen offset:0 sc1\n"
"buffer_load_dwordx4 v[244:247], v115, s[8:11], s34 offen offset:1024 sc1\n"
"buffer_load_dwordx4 v[248:251], v115, s[8:11], s34 offen offset:2048 sc1\n"
"buffer_load_dwordx4 v[252:255], v115, s[8:11], s34 offen offset:3072 sc1\n"
"s_waitcnt vmcnt(11)\n"
"v_cmp_eq_u32 vcc, -1, v208\n"
"s_cbranch_vccnz L_xs20\n"
"s_branch L_xr20\n"
"L_xs21:\n"
"s_add_u32 s34, s40, 20480\n"
"buffer_load_dwordx4 v[212:215], v115, s[8:11], s34 offen offset:1024 sc1\n"
"buffer_load_dwordx4 v[216:219], v115, s[8:11], s34 offen offset:2048 sc1\n"
"buffer_load_dwordx4 v[220:223], v115, s[8:11], s34 offen offset:3072 sc1\n"
"s_add_u32 s34, s40, 24576\n"
"buffer_load_dwordx4 v[224:227], v115, s[8:11], s34 offen offset:0 sc1\n"
"buffer_load_dwordx4 v[228:231], v115, s[8:11], s34 offen offset:1024 sc1\n"
"buffer_load_dwordx4 v[232:235], v115, s[8:11], s34 offen offset:2048 sc1\n"
"buffer_load_dwordx4 v[236:239], v115, s[8:11], s34 offen offset:3072 sc1\n"
"s_add_u32 s34, s40, 28672\n"
"buffer_load_dwordx4 v[240:243], v115, s[8:11], s34 offen offset:0 sc1\n"
"buffer_load_dwordx4 v[244:247], v115, s[8:11], s34 offen offset:1024 sc1\n"
"buffer_load_dwordx4 v[248:251], v115, s[8:11], s34 offen offset:2048 sc1\n"
"buffer_load_dwordx4 v[252:255], v115, s[8:11], s34 offen offset:3072 sc1\n"
"s_waitcnt vmcnt(10)\n"
"v_cmp_eq_u32 vcc, -1, v212\n"
"s_cbranch_vccnz L_xs21\n"
"s_branch L_xr21\n"
"L_xs22:\n"
"s_add_u32 s34, s40, 20480\n"
"buffer_load_dwordx4 v[216:219], v115, s[8:11], s34 offen offset:2048 sc1\n"
"buffer_load_dwordx4 v[220:223], v115, s[8:11], s34 offen offset:3072 sc1\n"
"s_add_u32 s34, s40, 24576\n"
"buffer_load_dwordx4 v[224:227], v115, s[8:11], s34 offen offset:0 sc1\n"
"buffer_load_dwordx4 v[228:231], v115, s[8:11], s34 offen offset:1024 sc1\n"
"buffer_load_dwordx4 v[232:235], v115, s[8:11], s34 offen offset:2048 sc1\n"
"buffer_load_dwordx4 v[236:239], v115, s[8:11], s34 offen offset:3072 sc1\n"
"s_add_u32 s34, s40, 28672\n"
"buffer_load_dwordx4 v[240:243], v115, s[8:11], s34 offen offset:0 sc1\n"
"buffer_load_dwordx4 v[244:247], v115, s[8:11], s34 offen offset:1024 sc1\n"
"buffer_load_dwordx4 v[248:251], v115, s[8:11], s34 offen offset:2048 sc1\n"
"buffer_load_dwordx4 v[252:255], v115, s[8:11], s34 offen offset:3072 sc1\n"
"buffer_load_dword v120, v115, s[8:11], s34 offen sc1\n"
"s_waitcnt vmcnt(10)\n"
"v_cmp_eq_u32 vcc, -1, v216\n"
"s_cbranch_vccnz L_xs22\n"
"s_branch L_xr22\n"
"L_xs23:\n"
"s_add_u32 s34, s40, 20480\n"
"buffer_load_dwordx4 v[220:223], v115, s[8:11], s34 offen offset:3072 sc1\n"
"s_add_u32 s34, s40, 24576\n"
"buffer_load_dwordx4 v[224:227], v115, s[8:11], s34 offen offset:0 sc1\n"
"buffer_load_dwordx4 v[228:231], v115, s[8:11], s34 offen offset:1024 sc1\n"
"buffer_load_dwordx4 v[232:235], v115, s[8:11], s34 offen offset:2048 sc1\n"
"buffer_load_dwordx4 v[236:239], v115, s[8:11], s34 offen offset:3072 sc1\n"
"s_add_u32 s34, s40, 28672\n"
"buffer_load_dwordx4 v[240:243], v115, s[8:11], s34 offen offset:0 sc1\n"
"buffer_load_dwordx4 v[244:247], v115, s[8:11], s34 offen offset:1024 sc1\n"
"buffer_load_dwordx4 v[248:251], v115, s[8:11], s34 offen offset:2048 sc1\n"
"buffer_load_dwordx4 v[252:255], v115, s[8:11], s34 offen offset:3072 sc1\n"
"buffer_load_dword v120, v115, s[8:11], s34 offen sc1\n"
"buffer_load_dword v120, v115, s[8:11], s34 offen sc1\n"
"s_waitcnt vmcnt(10)\n"
"v_cmp_eq_u32 vcc, -1, v220\n"
"s_cbranch_vccnz L_xs23\n"
"s_branch L_xr23\n"
"L_xs24:\n"
"s_add_u32 s34, s40, 24576\n"
"buffer_load_dwordx4 v[224:227], v115, s[8:11], s34 offen offset:0 sc1\n"
"buffer_load_dwordx4 v[228:231], v115, s[8:11], s34 offen offset:1024 sc1\n"
"buffer_load_dwordx4 v[232:235], v115, s[8:11], s34 offen offset:2048 sc1\n"
"buffer_load_dwordx4 v[236:239], v115, s[8:11], s34 offen offset:3072 sc1\n"
"s_add_u32 s34, s40, 28672\n"
"buffer_load_dwordx4 v[240:243], v115, s[8:11], s34 offen offset:0 sc1\n"
"buffer_load_dwordx4 v[244:247], v115, s[8:11], s34 offen offset:1024 sc1\n"
"buffer_load_dwordx4 v[248:251], v115, s[8:11], s34 offen offset:2048 sc1\n"
"buffer_load_dwordx4 v[252:255], v115, s[8:11], s34 offen offset:3072 sc1\n"
"buffer_load_dword v120, v115, s[8:11], s34 offen sc1\n"
"buffer_load_dword v120, v115, s[8:11], s34 offen sc1\n"
"buffer_load_dword v120, v115, s[8:11], s34 offen sc1\n"
"s_waitcnt vmcnt(10)\n"
"v_cmp_eq_u32 vcc, -1, v224\n"
"s_cbranch_vccnz L_xs24\n"
"s_branch L_xr24\n"
"L_xs25:\n"
"s_add_u32 s34, s40, 24576\n"
"buffer_load_dwordx4 v[228:231], v115, s[8:11], s34 offen offset:1024 sc1\n"
"buffer_load_dwordx4 v[232:235], v115, s[8:11], s34 offen offset:2048 sc1\n"
"buffer_load_dwordx4 v[236:239], v115, s[8:11], s34 offen offset:3072 sc1\n"
"s_add_u32 s34, s40, 28672\n"
"buffer_load_dwordx4 v[240:243], v115, s[8:11], s34 offen offset:0 sc1\n"
"buffer_load_dwordx4 v[244:247], v115, s[8:11], s34 offen offset:1024 sc1\n"
"buffer_load_dwordx4 v[248:251], v115, s[8:11], s34 offen offset:2048 sc1\n"
"buffer_load_dwordx4 v[252:255], v115, s[8:11], s34 offen offset:3072 sc1\n"
"buffer_load_dword v120, v115, s[8:11], s34 offen sc1\n"
"buffer_load_dword v120, v115, s[8:11], s34 offen sc1\n"
"buffer_load_dword v120, v115, s[8:11], s34 offen sc1\n"
"buffer_load_dword v120, v115, s[8:11], s34 offen sc1\n"
"s_waitcnt vmcnt(10)\n"
"v_cmp_eq_u32 vcc, -1, v228\n"
"s_cbranch_vccnz L_xs25\n"
"s_branch L_xr25\n"
"L_xs26:\n"
"s_add_u32 s34, s40, 24576\n"
"buffer_load_dwordx4 v[232:235], v115, s[8:11], s34 offen offset:2048 sc1\n"
"buffer_load_dwordx4 v[236:239], v115, s[8:11], s34 offen offset:3072 sc1\n"
"s_add_u32 s34, s40, 28672\n"
"buffer_load_dwordx4 v[240:243], v115, s[8:11], s34 offen offset:0 sc1\n"
"buffer_load_dwordx4 v[244:247], v115, s[8:11], s34 offen offset:1024 sc1\n"
"buffer_load_dwordx4 v[248:251], v115, s[8:11], s34 offen offset:2048 sc1\n"
"buffer_load_dwordx4 v[252:255], v115, s[8:11], s34 offen offset:3072 sc1\n"
"buffer_load_dword v120, v115, s[8:11], s34 offen sc1\n"
"buffer_load_dword v120, v115, s[8:11], s34 offen sc1\n"
"buffer_load_dword v120, v115, s[8:11], s34 offen sc1\n"
"buffer_load_dword v120, v115, s[8:11], s34 offen sc1\n"
"buffer_load_dword v120, v115, s[8:11], s34 offen sc1\n"
"s_waitcnt vmcnt(10)\n"
"v_cmp_eq_u32 vcc, -1, v232\n"
"s_cbranch_vccnz L_xs26\n"
"s_branch L_xr26\n"
"L_xs27:\n"
"s_add_u32 s34, s40, 24576\n"
"buffer_load_dwordx4 v[236:239], v115, s[8:11], s34 offen offset:3072 sc1\n"
"s_add_u32 s34, s40, 28672\n"
"buffer_load_dwordx4 v[240:243], v115, s[8:11], s34 offen offset:0 sc1\n"
"buffer_load_dwordx4 v[244:247], v115, s[8:11], s34 offen offset:1024 sc1\n"
"buffer_load_dwordx4 v[248:251], v115, s[8:11], s34 offen offset:2048 sc1\n"
"buffer_load_dwordx4 v[252:255], v115, s[8:11], s34 offen offset:3072 sc1\n"
"buffer_load_dword v120, v115, s[8:11], s34 offen sc1\n"
"buffer_load_dword v120, v115, s[8:11], s34 offen sc1\n"
"buffer_load_dword v120, v115, s[8:11], s34 offen sc1\n"
"buffer_load_dword v120, v115, s[8:11], s34 offen sc1\n"
"buffer_load_dword v120, v115, s[8:11], s34 offen sc1\n"
"s_waitcnt vmcnt(9)\n"
"v_cmp_eq_u32 vcc, -1, v236\n"
"s_cbranch_vccnz L_xs27\n"
"s_branch L_xr27\n"
"L_xs28:\n"
"s_add_u32 s34, s40, 28672\n"
"buffer_load_dwordx4 v[240:243], v115, s[8:11], s34 offen offset:0 sc1\n"
"buffer_load_dwordx4 v[244:247], v115, s[8:11], s34 offen offset:1024 sc1\n"
"buffer_load_dwordx4 v[248:251], v115, s[8:11], s34 offen offset:2048 sc1\n"
"buffer_load_dwordx4 v[252:255], v115, s[8:11], s34 offen offset:3072 sc1\n"
"buffer_load_dword v120, v115, s[8:11], s34 offen sc1\n"
"buffer_load_dword v120, v115, s[8:11], s34 offen sc1\n"
"buffer_load_dword v120, v115, s[8:11], s34 offen sc1\n"
"buffer_load_dword v120, v115, s[8:11], s34 offen sc1\n"
"buffer_load_dword v120, v115, s[8:11], s34 offen sc1\n"
"s_waitcnt vmcnt(8)\n"
"v_cmp_eq_u32 vcc, -1, v240\n"
"s_cbranch_vccnz L_xs28\n"
"s_branch L_xr28\n"
"L_xs29:\n"
"s_add_u32 s34, s40, 28672\n"
"buffer_load_dwordx4 v[244:247], v115, s[8:11], s34 offen offset:1024 sc1\n"
"buffer_load_dwordx4 v[248:251], v115, s[8:11], s34 offen offset:2048 sc1\n"
"buffer_load_dwordx4 v[252:255], v115, s[8:11], s34 offen offset:3072 sc1\n"
"buffer_load_dword v120, v115, s[8:11], s34 offen sc1\n"
"buffer_load_dword v120, v115, s[8:11], s34 offen sc1\n"
"buffer_load_dword v120, v115, s[8:11], s34 offen sc1\n"
"buffer_load_dword v120, v115, s[8:11], s34 offen sc1\n"
"buffer_load_dword v120, v115, s[8:11], s34 offen sc1\n"
"s_waitcnt vmcnt(7)\n"
"v_cmp_eq_u32 vcc, -1, v244\n"
"s_cbranch_vccnz L_xs29\n"
"s_branch L_xr29\n"
"L_xs30:\n"
"s_add_u32 s34, s40, 28672\n"
"buffer_load_dwordx4 v[248:251], v115, s[8:11], s34 offen offset:2048 sc1\n"
"buffer_load_dwordx4 v[252:255], v115, s[8:11], s34 offen offset:3072 sc1\n"
"buffer_load_dword v120, v115, s[8:11], s34 offen sc1\n"
"buffer_load_dword v120, v115, s[8:11], s34 offen sc1\n"
"buffer_load_dword v120, v115, s[8:11], s34 offen sc1\n"
"buffer_load_dword v120, v115, s[8:11], s34 offen sc1\n"
"buffer_load_dword v120, v115, s[8:11], s34 offen sc1\n"
"s_waitcnt vmcnt(6)\n"
"v_cmp_eq_u32 vcc, -1, v248\n"
"s_cbranch_vccnz L_xs30\n"
"s_branch L_xr30\n"
"L_xs31:\n"
"s_add_u32 s34, s40, 28672\n"
"buffer_load_dwordx4 v[252:255], v115, s[8:11], s34 offen offset:3072 sc1\n"
"buffer_load_dword v120, v115, s[8:11], s34 offen sc1\n"
"buffer_load_dword v120, v115, s[8:11], s34 offen sc1\n"
"buffer_load_dword v120, v115, s[8:11], s34 offen sc1\n"
"buffer_load_dword v120, v115, s[8:11], s34 offen sc1\n"
"buffer_load_dword v120, v115, s[8:11], s34 offen sc1\n"
"s_waitcnt vmcnt(5)\n"
"v_cmp_eq_u32 vcc, -1, v252\n"
"s_cbranch_vccnz L_xs31\n"
"s_branch L_xr31\n"
"L_hs:\n"
"s_mov_b32 m0, s50\n"
"s_add_u32 s35, s41, 3072\n"
"buffer_load_dwordx4 v40, s[8:11], s41 offen offset:0 sc1 lds\n"
"buffer_load_dwordx4 v40, s[8:11], s35 offen offset:1024 sc1 lds\n"
"s_waitcnt vmcnt(0)\n"
"ds_read_b128 v[80:83], v43 offset:0\n"
"ds_read_b128 v[84:87], v43 offset:1024\n"
"s_waitcnt lgkmcnt(0)\n"
"v_max_u32 v80, v80, v84\n"
"v_cmp_eq_u32 vcc, -1, v80\n"
"s_cbranch_vccnz L_hs\n"
"s_branch L_hr\n"
"L_ss:\n"
"buffer_load_dwordx4 v[32:35], v41, s[8:11], s42 offen sc1\n"
"buffer_load_dwordx2 v[36:37], v42, s[8:11], s43 offen sc1\n"
"s_waitcnt vmcnt(1)\n"
"v_max3_u32 v93, v32, v33, v34\n"
"s_waitcnt vmcnt(0)\n"
"v_max3_u32 v93, v93, v35, v36\n"
"v_max_u32 v93, v93, v37\n"
"v_cmp_eq_u32 vcc, -1, v93\n"
"s_cbranch_vccnz L_ss\n"
"s_branch L_sr\n"
"L_os0:\n"
"s_mov_b32 m0, s51\n"
"s_nop 0\n"
"buffer_load_dwordx4 v115, s[8:11], s45 offen offset:0 sc1 lds\n"
"buffer_load_dwordx4 v115, s[8:11], s45 offen offset:1024 sc1 lds\n"
"buffer_load_dwordx4 v115, s[8:11], s45 offen offset:2048 sc1 lds\n"
"buffer_load_dwordx4 v115, s[8:11], s45 offen offset:3072 sc1 lds\n"
"s_add_u32 m0, s51, 16384\n"
"s_add_u32 s35, s45, 16384\n"
"buffer_load_dwordx4 v115, s[8:11], s35 offen offset:0 sc1 lds\n"
"buffer_load_dwordx4 v115, s[8:11], s35 offen offset:1024 sc1 lds\n"
"buffer_load_dwordx4 v115, s[8:11], s35 offen offset:2048 sc1 lds\n"
"buffer_load_dwordx4 v115, s[8:11], s35 offen offset:3072 sc1 lds\n"
"s_waitcnt vmcnt(0)\n"
"ds_read_b32 v61, v56\n"
"ds_read_b32 v62, v56 offset:16384\n"
"s_waitcnt lgkmcnt(0)\n"
"v_max_u32 v60, v61, v62\n"
"v_cmp_eq_u32 vcc, -1, v60\n"
"s_cbranch_vccnz L_os0\n"
"s_branch L_or0\n"
"L_ot0:\n"
"s_mov_b32 s3, m0\n"
"s_add_u32 m0, s51, 16384\n"
"s_add_u32 s35, s45, 16384\n"
"buffer_load_dwordx4 v115, s[8:11], s35 offen offset:0 sc1 lds\n"
"buffer_load_dwordx4 v115, s[8:11], s35 offen offset:1024 sc1 lds\n"
"buffer_load_dwordx4 v115, s[8:11], s35 offen offset:2048 sc1 lds\n"
"buffer_load_dwordx4 v115, s[8:11], s35 offen offset:3072 sc1 lds\n"
"s_mov_b32 m0, s3\n"
"s_waitcnt vmcnt(0)\n"
"ds_read_b32 v61, v56 offset:16384\n"
"s_waitcnt lgkmcnt(0)\n"
"v_cmp_eq_u32 vcc, -1, v61\n"
"s_cbranch_vccnz L_ot0\n"
"s_branch L_ou0\n"
"L_os1:\n"
"s_mov_b32 m0, s51\n"
"s_nop 0\n"
"buffer_load_dwordx4 v115, s[8:11], s45 offen offset:0 sc1 lds\n"
"buffer_load_dwordx4 v115, s[8:11], s45 offen offset:1024 sc1 lds\n"
"buffer_load_dwordx4 v115, s[8:11], s45 offen offset:2048 sc1 lds\n"
"buffer_load_dwordx4 v115, s[8:11], s45 offen offset:3072 sc1 lds\n"
"s_add_u32 m0, s51, 16384\n"
"s_add_u32 s35, s45, 16384\n"
"buffer_load_dwordx4 v115, s[8:11], s35 offen offset:0 sc1 lds\n"
"buffer_load_dwordx4 v115, s[8:11], s35 offen offset:1024 sc1 lds\n"
"buffer_load_dwordx4 v115, s[8:11], s35 offen offset:2048 sc1 lds\n"
"buffer_load_dwordx4 v115, s[8:11], s35 offen offset:3072 sc1 lds\n"
"s_waitcnt vmcnt(0)\n"
"ds_read_b32 v61, v56\n"
"ds_read_b32 v62, v56 offset:16384\n"
"s_waitcnt lgkmcnt(0)\n"
"v_max_u32 v60, v61, v62\n"
"v_cmp_eq_u32 vcc, -1, v60\n"
"s_cbranch_vccnz L_os1\n"
"s_branch L_or1\n"
"L_ot1:\n"
"s_mov_b32 s3, m0\n"
"s_add_u32 m0, s51, 16384\n"
"s_add_u32 s35, s45, 16384\n"
"buffer_load_dwordx4 v115, s[8:11], s35 offen offset:0 sc1 lds\n"
"buffer_load_dwordx4 v115, s[8:11], s35 offen offset:1024 sc1 lds\n"
"buffer_load_dwordx4 v115, s[8:11], s35 offen offset:2048 sc1 lds\n"
"buffer_load_dwordx4 v115, s[8:11], s35 offen offset:3072 sc1 lds\n"
"s_mov_b32 m0, s3\n"
"s_waitcnt vmcnt(0)\n"
"ds_read_b32 v61, v56 offset:16384\n"
"s_waitcnt lgkmcnt(0)\n"
"v_cmp_eq_u32 vcc, -1, v61\n"
"s_cbranch_vccnz L_ot1\n"
"s_branch L_ou1\n"
"L_os2:\n"
"s_mov_b32 m0, s51\n"
"s_nop 0\n"
"buffer_load_dwordx4 v115, s[8:11], s45 offen offset:0 sc1 lds\n"
"buffer_load_dwordx4 v115, s[8:11], s45 offen offset:1024 sc1 lds\n"
"buffer_load_dwordx4 v115, s[8:11], s45 offen offset:2048 sc1 lds\n"
"buffer_load_dwordx4 v115, s[8:11], s45 offen offset:3072 sc1 lds\n"
"s_add_u32 m0, s51, 16384\n"
"s_add_u32 s35, s45, 16384\n"
"buffer_load_dwordx4 v115, s[8:11], s35 offen offset:0 sc1 lds\n"
"buffer_load_dwordx4 v115, s[8:11], s35 offen offset:1024 sc1 lds\n"
"buffer_load_dwordx4 v115, s[8:11], s35 offen offset:2048 sc1 lds\n"
"buffer_load_dwordx4 v115, s[8:11], s35 offen offset:3072 sc1 lds\n"
"s_waitcnt vmcnt(0)\n"
"ds_read_b32 v61, v56\n"
"ds_read_b32 v62, v56 offset:16384\n"
"s_waitcnt lgkmcnt(0)\n"
"v_max_u32 v60, v61, v62\n"
"v_cmp_eq_u32 vcc, -1, v60\n"
"s_cbranch_vccnz L_os2\n"
"s_branch L_or2\n"
"L_ot2:\n"
"s_mov_b32 s3, m0\n"
"s_add_u32 m0, s51, 16384\n"
"s_add_u32 s35, s45, 16384\n"
"buffer_load_dwordx4 v115, s[8:11], s35 offen offset:0 sc1 lds\n"
"buffer_load_dwordx4 v115, s[8:11], s35 offen offset:1024 sc1 lds\n"
"buffer_load_dwordx4 v115, s[8:11], s35 offen offset:2048 sc1 lds\n"
"buffer_load_dwordx4 v115, s[8:11], s35 offen offset:3072 sc1 lds\n"
"s_mov_b32 m0, s3\n"
"s_waitcnt vmcnt(0)\n"
"ds_read_b32 v61, v56 offset:16384\n"
"s_waitcnt lgkmcnt(0)\n"
"v_cmp_eq_u32 vcc, -1, v61\n"
"s_cbranch_vccnz L_ot2\n"
"s_branch L_ou2\n"
"L_os3:\n"
"s_mov_b32 m0, s51\n"
"s_nop 0\n"
"buffer_load_dwordx4 v115, s[8:11], s45 offen offset:0 sc1 lds\n"
"buffer_load_dwordx4 v115, s[8:11], s45 offen offset:1024 sc1 lds\n"
"buffer_load_dwordx4 v115, s[8:11], s45 offen offset:2048 sc1 lds\n"
"buffer_load_dwordx4 v115, s[8:11], s45 offen offset:3072 sc1 lds\n"
"s_add_u32 m0, s51, 16384\n"
"s_add_u32 s35, s45, 16384\n"
"buffer_load_dwordx4 v115, s[8:11], s35 offen offset:0 sc1 lds\n"
"buffer_load_dwordx4 v115, s[8:11], s35 offen offset:1024 sc1 lds\n"
"buffer_load_dwordx4 v115, s[8:11], s35 offen offset:2048 sc1 lds\n"
"buffer_load_dwordx4 v115, s[8:11], s35 offen offset:3072 sc1 lds\n"
"s_waitcnt vmcnt(0)\n"
"ds_read_b32 v61, v56\n"
"ds_read_b32 v62, v56 offset:16384\n"
"s_waitcnt lgkmcnt(0)\n"
"v_max_u32 v60, v61, v62\n"
"v_cmp_eq_u32 vcc, -1, v60\n"
"s_cbranch_vccnz L_os3\n"
"s_branch L_or3\n"
"L_ot3:\n"
"s_mov_b32 s3, m0\n"
"s_add_u32 m0, s51, 16384\n"
"s_add_u32 s35, s45, 16384\n"
"buffer_load_dwordx4 v115, s[8:11], s35 offen offset:0 sc1 lds\n"
"buffer_load_dwordx4 v115, s[8:11], s35 offen offset:1024 sc1 lds\n"
"buffer_load_dwordx4 v115, s[8:11], s35 offen offset:2048 sc1 lds\n"
"buffer_load_dwordx4 v115, s[8:11], s35 offen offset:3072 sc1 lds\n"
"s_mov_b32 m0, s3\n"
"s_waitcnt vmcnt(0)\n"
"ds_read_b32 v61, v56 offset:16384\n"
"s_waitcnt lgkmcnt(0)\n"
"v_cmp_eq_u32 vcc, -1, v61\n"
"s_cbranch_vccnz L_ot3\n"
"s_branch L_ou3\n"
"L_hc12_loop:\n"
"s_mov_b64 exec, s[74:75]\n"
"buffer_load_dwordx2 v[200:201], v206, s[8:11], 0 offen sc1\n"
"s_mov_b64 exec, s[76:77]\n"
"buffer_load_dwordx2 v[202:203], v207, s[8:11], 0 offen sc1\n"
"s_mov_b64 exec, -1\n"
"s_mov_b64 exec, s[76:77]\n"
"s_waitcnt vmcnt(0)\n"
"v_cmp_ne_u32 s[34:35], v205, v203\n"
"v_cmp_ne_u32 vcc, v202, v210\n"
"s_and_b64 exec, vcc, s[34:35]\n"
"s_cbranch_execz L_hc12b_noack\n"
"v_mov_b32 v203, v205\n"
"v_mov_b32 v210, v202\n"
"v_accvgpr_write_b32 a204, v202\n"
"buffer_store_dwordx2 v[202:203], v209, s[8:11], s36 offen offset:512 sc1\n"
"L_hc12b_noack:\n"
"s_mov_b64 exec, s[74:75]\n"
"v_cmp_ne_u32 vcc, v200, v204\n"
"s_mov_b64 exec, -1\n"
"s_cmp_eq_u64 vcc, 0\n"
"s_cbranch_scc0 L_hc12_loop\n"
"s_branch L_hc12_done\n"
"L_hc15_loop:\n"
"s_mov_b64 exec, s[76:77]\n"
"buffer_load_dwordx2 v[210:211], v215, s[8:11], 0 offen sc1\n"
"s_mov_b64 exec, -1\n"
"s_mov_b64 exec, s[76:77]\n"
"s_waitcnt vmcnt(0)\n"
"v_cmp_ne_u32 s[34:35], v213, v211\n"
"v_cmp_ne_u32 vcc, v210, v218\n"
"s_and_b64 exec, vcc, s[34:35]\n"
"s_cbranch_execz L_hc15b_noack\n"
"v_mov_b32 v211, v213\n"
"v_mov_b32 v218, v210\n"
"v_accvgpr_write_b32 a204, v210\n"
"buffer_store_dwordx2 v[210:211], v217, s[8:11], s36 offen offset:512 sc1\n"
"L_hc15b_noack:\n"
"s_mov_b64 exec, -1\n"
"s_cmp_eq_u64 s[34:35], 0\n"
"s_cbranch_scc0 L_hc15_loop\n"
"s_branch L_hc15_done\n"
"L_fail_o0:\n"
"s_nop 8\n"
"v_lshlrev_b32 v32, 16, v104\n"
"v_and_b32 v33, 0xffff0000, v104\n"
"v_lshlrev_b32 v34, 16, v105\n"
"v_and_b32 v35, 0xffff0000, v105\n"
"v_lshlrev_b32 v36, 16, v106\n"
"v_and_b32 v37, 0xffff0000, v106\n"
"v_lshlrev_b32 v38, 16, v107\n"
"v_and_b32 v39, 0xffff0000, v107\n"
"v_lshlrev_b32 v40, 16, v108\n"
"v_and_b32 v41, 0xffff0000, v108\n"
"v_lshlrev_b32 v42, 16, v109\n"
"v_and_b32 v43, 0xffff0000, v109\n"
"v_lshlrev_b32 v44, 16, v110\n"
"v_and_b32 v45, 0xffff0000, v110\n"
"v_lshlrev_b32 v46, 16, v111\n"
"v_and_b32 v47, 0xffff0000, v111\n"
"s_waitcnt lgkmcnt(0)\n"
"s_barrier\n"
"s_mov_b32 m0, s51\n"
"s_nop 0\n"
"buffer_load_dwordx4 v115, s[8:11], s45 offen offset:0 sc1 lds\n"
"buffer_load_dwordx4 v115, s[8:11], s45 offen offset:1024 sc1 lds\n"
"buffer_load_dwordx4 v115, s[8:11], s45 offen offset:2048 sc1 lds\n"
"buffer_load_dwordx4 v115, s[8:11], s45 offen offset:3072 sc1 lds\n"
"s_add_u32 m0, s51, 16384\n"
"s_add_u32 s35, s45, 16384\n"
"buffer_load_dwordx4 v115, s[8:11], s35 offen offset:0 sc1 lds\n"
"buffer_load_dwordx4 v115, s[8:11], s35 offen offset:1024 sc1 lds\n"
"buffer_load_dwordx4 v115, s[8:11], s35 offen offset:2048 sc1 lds\n"
"buffer_load_dwordx4 v115, s[8:11], s35 offen offset:3072 sc1 lds\n"
"s_waitcnt vmcnt(0)\n"
"s_waitcnt lgkmcnt(0)\n"
"s_barrier\n"
"ds_read_b128 v[128:131], v116 offset:0\n"
"ds_read_b128 v[132:135], v116 offset:1024\n"
"ds_read_b128 v[136:139], v116 offset:2048\n"
"ds_read_b128 v[140:143], v116 offset:3072\n"
"v_mov_b32 v92, 0\n"
"s_waitcnt lgkmcnt(3)\n"
"v_mfma_f32_32x32x16_bf16 v[32:47], a[0:3], v[128:131], v[32:47]\n"
"ds_read_b128 v[144:147], v116 offset:4096\n"
"s_waitcnt lgkmcnt(3)\n"
"v_mfma_f32_32x32x16_bf16 v[32:47], a[4:7], v[132:135], v[32:47]\n"
"ds_read_b128 v[148:151], v116 offset:5120\n"
"v_max3_u32 v92, v92, v128, v132\n"
"s_waitcnt lgkmcnt(3)\n"
"v_mfma_f32_32x32x16_bf16 v[32:47], a[8:11], v[136:139], v[32:47]\n"
"ds_read_b128 v[152:155], v116 offset:6144\n"
"s_waitcnt lgkmcnt(3)\n"
"v_mfma_f32_32x32x16_bf16 v[32:47], a[12:15], v[140:143], v[32:47]\n"
"ds_read_b128 v[156:159], v116 offset:7168\n"
"v_max3_u32 v92, v92, v136, v140\n"
"s_waitcnt lgkmcnt(3)\n"
"v_mfma_f32_32x32x16_bf16 v[32:47], a[16:19], v[144:147], v[32:47]\n"
"ds_read_b128 v[160:163], v116 offset:8192\n"
"s_waitcnt lgkmcnt(3)\n"
"v_mfma_f32_32x32x16_bf16 v[32:47], a[20:23], v[148:151], v[32:47]\n"
"ds_read_b128 v[164:167], v116 offset:9216\n"
"v_max3_u32 v92, v92, v144, v148\n"
"s_waitcnt lgkmcnt(3)\n"
"v_mfma_f32_32x32x16_bf16 v[32:47], a[24:27], v[152:155], v[32:47]\n"
"ds_read_b128 v[168:171], v116 offset:10240\n"
"s_waitcnt lgkmcnt(3)\n"
"v_mfma_f32_32x32x16_bf16 v[32:47], a[28:31], v[156:159], v[32:47]\n"
"ds_read_b128 v[172:175], v116 offset:11264\n"
"v_max3_u32 v92, v92, v152, v156\n"
"s_waitcnt lgkmcnt(3)\n"
"v_mfma_f32_32x32x16_bf16 v[32:47], a[32:35], v[160:163], v[32:47]\n"
"ds_read_b128 v[176:179], v116 offset:12288\n"
"s_waitcnt lgkmcnt(3)\n"
"v_mfma_f32_32x32x16_bf16 v[32:47], a[36:39], v[164:167], v[32:47]\n"
"ds_read_b128 v[180:183], v116 offset:13312\n"
"v_max3_u32 v92, v92, v160, v164\n"
"s_waitcnt lgkmcnt(3)\n"
"v_mfma_f32_32x32x16_bf16 v[32:47], a[40:43], v[168:171], v[32:47]\n"
"ds_read_b128 v[184:187], v116 offset:14336\n"
"s_waitcnt lgkmcnt(3)\n"
"v_mfma_f32_32x32x16_bf16 v[32:47], a[44:47], v[172:175], v[32:47]\n"
"ds_read_b128 v[188:191], v116 offset:15360\n"
"v_max3_u32 v92, v92, v168, v172\n"
"s_waitcnt lgkmcnt(3)\n"
"v_mfma_f32_32x32x16_bf16 v[32:47], a[48:51], v[176:179], v[32:47]\n"
"ds_read_b128 v[192:195], v116 offset:16384\n"
"s_waitcnt lgkmcnt(3)\n"
"v_mfma_f32_32x32x16_bf16 v[32:47], a[52:55], v[180:183], v[32:47]\n"
"ds_read_b128 v[196:199], v116 offset:17408\n"
"v_max3_u32 v92, v92, v176, v180\n"
"s_waitcnt lgkmcnt(3)\n"
"v_mfma_f32_32x32x16_bf16 v[32:47], a[56:59], v[184:187], v[32:47]\n"
"ds_read_b128 v[200:203], v116 offset:18432\n"
"s_waitcnt lgkmcnt(3)\n"
"v_mfma_f32_32x32x16_bf16 v[32:47], a[60:63], v[188:191], v[32:47]\n"
"ds_read_b128 v[204:207], v116 offset:19456\n"
"v_max3_u32 v92, v92, v184, v188\n"
"s_waitcnt lgkmcnt(3)\n"
"v_mfma_f32_32x32x16_bf16 v[32:47], a[64:67], v[192:195], v[32:47]\n"
"ds_read_b128 v[208:211], v116 offset:20480\n"
"s_waitcnt lgkmcnt(3)\n"
"v_mfma_f32_32x32x16_bf16 v[32:47], a[68:71], v[196:199], v[32:47]\n"
"ds_read_b128 v[212:215], v116 offset:21504\n"
"v_max3_u32 v92, v92, v192, v196\n"
"s_waitcnt lgkmcnt(3)\n"
"v_mfma_f32_32x32x16_bf16 v[32:47], a[72:75], v[200:203], v[32:47]\n"
"ds_read_b128 v[216:219], v116 offset:22528\n"
"s_waitcnt lgkmcnt(3)\n"
"v_mfma_f32_32x32x16_bf16 v[32:47], a[76:79], v[204:207], v[32:47]\n"
"ds_read_b128 v[220:223], v116 offset:23552\n"
"v_max3_u32 v92, v92, v200, v204\n"
"s_waitcnt lgkmcnt(3)\n"
"v_mfma_f32_32x32x16_bf16 v[32:47], a[80:83], v[208:211], v[32:47]\n"
"ds_read_b128 v[224:227], v116 offset:24576\n"
"s_waitcnt lgkmcnt(3)\n"
"v_mfma_f32_32x32x16_bf16 v[32:47], a[84:87], v[212:215], v[32:47]\n"
"ds_read_b128 v[228:231], v116 offset:25600\n"
"v_max3_u32 v92, v92, v208, v212\n"
"s_waitcnt lgkmcnt(3)\n"
"v_mfma_f32_32x32x16_bf16 v[32:47], a[88:91], v[216:219], v[32:47]\n"
"ds_read_b128 v[232:235], v116 offset:26624\n"
"s_waitcnt lgkmcnt(3)\n"
"v_mfma_f32_32x32x16_bf16 v[32:47], a[92:95], v[220:223], v[32:47]\n"
"ds_read_b128 v[236:239], v116 offset:27648\n"
"v_max3_u32 v92, v92, v216, v220\n"
"s_waitcnt lgkmcnt(3)\n"
"v_mfma_f32_32x32x16_bf16 v[32:47], a[96:99], v[224:227], v[32:47]\n"
"ds_read_b128 v[240:243], v116 offset:28672\n"
"s_waitcnt lgkmcnt(3)\n"
"v_mfma_f32_32x32x16_bf16 v[32:47], a[100:103], v[228:231], v[32:47]\n"
"ds_read_b128 v[244:247], v116 offset:29696\n"
"v_max3_u32 v92, v92, v224, v228\n"
"s_waitcnt lgkmcnt(3)\n"
"v_mfma_f32_32x32x16_bf16 v[32:47], a[104:107], v[232:235], v[32:47]\n"
"ds_read_b128 v[248:251], v116 offset:30720\n"
"s_waitcnt lgkmcnt(3)\n"
"v_mfma_f32_32x32x16_bf16 v[32:47], a[108:111], v[236:239], v[32:47]\n"
"ds_read_b128 v[252:255], v116 offset:31744\n"
"v_max3_u32 v92, v92, v232, v236\n"
"s_waitcnt lgkmcnt(3)\n"
"v_mfma_f32_32x32x16_bf16 v[32:47], a[112:115], v[240:243], v[32:47]\n"
"s_waitcnt lgkmcnt(2)\n"
"v_mfma_f32_32x32x16_bf16 v[32:47], a[116:119], v[244:247], v[32:47]\n"
"v_max3_u32 v92, v92, v240, v244\n"
"s_waitcnt lgkmcnt(1)\n"
"v_mfma_f32_32x32x16_bf16 v[32:47], a[120:123], v[248:251], v[32:47]\n"
"s_waitcnt lgkmcnt(0)\n"
"v_mfma_f32_32x32x16_bf16 v[32:47], a[124:127], v[252:255], v[32:47]\n"
"v_max3_u32 v92, v92, v248, v252\n"
"v_cmp_eq_u32 vcc, -1, v92\n"
"s_cbranch_vccnz L_fail_o0\n"
"s_branch L_ok_o0\n"
"L_fail_o1:\n"
"s_nop 8\n"
"v_lshlrev_b32 v32, 16, v104\n"
"v_and_b32 v33, 0xffff0000, v104\n"
"v_lshlrev_b32 v34, 16, v105\n"
"v_and_b32 v35, 0xffff0000, v105\n"
"v_lshlrev_b32 v36, 16, v106\n"
"v_and_b32 v37, 0xffff0000, v106\n"
"v_lshlrev_b32 v38, 16, v107\n"
"v_and_b32 v39, 0xffff0000, v107\n"
"v_lshlrev_b32 v40, 16, v108\n"
"v_and_b32 v41, 0xffff0000, v108\n"
"v_lshlrev_b32 v42, 16, v109\n"
"v_and_b32 v43, 0xffff0000, v109\n"
"v_lshlrev_b32 v44, 16, v110\n"
"v_and_b32 v45, 0xffff0000, v110\n"
"v_lshlrev_b32 v46, 16, v111\n"
"v_and_b32 v47, 0xffff0000, v111\n"
"s_waitcnt lgkmcnt(0)\n"
"s_barrier\n"
"s_mov_b32 m0, s51\n"
"s_nop 0\n"
"buffer_load_dwordx4 v115, s[8:11], s45 offen offset:0 sc1 lds\n"
"buffer_load_dwordx4 v115, s[8:11], s45 offen offset:1024 sc1 lds\n"
"buffer_load_dwordx4 v115, s[8:11], s45 offen offset:2048 sc1 lds\n"
"buffer_load_dwordx4 v115, s[8:11], s45 offen offset:3072 sc1 lds\n"
"s_add_u32 m0, s51, 16384\n"
"s_add_u32 s35, s45, 16384\n"
"buffer_load_dwordx4 v115, s[8:11], s35 offen offset:0 sc1 lds\n"
"buffer_load_dwordx4 v115, s[8:11], s35 offen offset:1024 sc1 lds\n"
"buffer_load_dwordx4 v115, s[8:11], s35 offen offset:2048 sc1 lds\n"
"buffer_load_dwordx4 v115, s[8:11], s35 offen offset:3072 sc1 lds\n"
"s_waitcnt vmcnt(0)\n"
"s_waitcnt lgkmcnt(0)\n"
"s_barrier\n"
"ds_read_b128 v[128:131], v116 offset:0\n"
"ds_read_b128 v[132:135], v116 offset:1024\n"
"ds_read_b128 v[136:139], v116 offset:2048\n"
"ds_read_b128 v[140:143], v116 offset:3072\n"
"v_mov_b32 v92, 0\n"
"s_waitcnt lgkmcnt(3)\n"
"v_mfma_f32_32x32x16_bf16 v[32:47], a[128:131], v[128:131], v[32:47]\n"
"ds_read_b128 v[144:147], v116 offset:4096\n"
"s_waitcnt lgkmcnt(3)\n"
"v_mfma_f32_32x32x16_bf16 v[32:47], a[132:135], v[132:135], v[32:47]\n"
"ds_read_b128 v[148:151], v116 offset:5120\n"
"v_max3_u32 v92, v92, v128, v132\n"
"s_waitcnt lgkmcnt(3)\n"
"v_mfma_f32_32x32x16_bf16 v[32:47], a[136:139], v[136:139], v[32:47]\n"
"ds_read_b128 v[152:155], v116 offset:6144\n"
"s_waitcnt lgkmcnt(3)\n"
"v_mfma_f32_32x32x16_bf16 v[32:47], a[140:143], v[140:143], v[32:47]\n"
"ds_read_b128 v[156:159], v116 offset:7168\n"
"v_max3_u32 v92, v92, v136, v140\n"
"s_waitcnt lgkmcnt(3)\n"
"v_mfma_f32_32x32x16_bf16 v[32:47], a[144:147], v[144:147], v[32:47]\n"
"ds_read_b128 v[160:163], v116 offset:8192\n"
"s_waitcnt lgkmcnt(3)\n"
"v_mfma_f32_32x32x16_bf16 v[32:47], a[148:151], v[148:151], v[32:47]\n"
"ds_read_b128 v[164:167], v116 offset:9216\n"
"v_max3_u32 v92, v92, v144, v148\n"
"s_waitcnt lgkmcnt(3)\n"
"v_mfma_f32_32x32x16_bf16 v[32:47], a[152:155], v[152:155], v[32:47]\n"
"ds_read_b128 v[168:171], v116 offset:10240\n"
"s_waitcnt lgkmcnt(3)\n"
"v_mfma_f32_32x32x16_bf16 v[32:47], a[156:159], v[156:159], v[32:47]\n"
"ds_read_b128 v[172:175], v116 offset:11264\n"
"v_max3_u32 v92, v92, v152, v156\n"
"s_waitcnt lgkmcnt(3)\n"
"v_mfma_f32_32x32x16_bf16 v[32:47], a[160:163], v[160:163], v[32:47]\n"
"ds_read_b128 v[176:179], v116 offset:12288\n"
"s_waitcnt lgkmcnt(3)\n"
"v_mfma_f32_32x32x16_bf16 v[32:47], a[164:167], v[164:167], v[32:47]\n"
"ds_read_b128 v[180:183], v116 offset:13312\n"
"v_max3_u32 v92, v92, v160, v164\n"
"s_waitcnt lgkmcnt(3)\n"
"v_mfma_f32_32x32x16_bf16 v[32:47], a[168:171], v[168:171], v[32:47]\n"
"ds_read_b128 v[184:187], v116 offset:14336\n"
"s_waitcnt lgkmcnt(3)\n"
"v_mfma_f32_32x32x16_bf16 v[32:47], a[172:175], v[172:175], v[32:47]\n"
"ds_read_b128 v[188:191], v116 offset:15360\n"
"v_max3_u32 v92, v92, v168, v172\n"
"s_waitcnt lgkmcnt(3)\n"
"v_mfma_f32_32x32x16_bf16 v[32:47], a[176:179], v[176:179], v[32:47]\n"
"ds_read_b128 v[192:195], v116 offset:16384\n"
"s_waitcnt lgkmcnt(3)\n"
"v_mfma_f32_32x32x16_bf16 v[32:47], a[180:183], v[180:183], v[32:47]\n"
"ds_read_b128 v[196:199], v116 offset:17408\n"
"v_max3_u32 v92, v92, v176, v180\n"
"s_waitcnt lgkmcnt(3)\n"
"v_mfma_f32_32x32x16_bf16 v[32:47], a[184:187], v[184:187], v[32:47]\n"
"ds_read_b128 v[200:203], v116 offset:18432\n"
"s_waitcnt lgkmcnt(3)\n"
"v_mfma_f32_32x32x16_bf16 v[32:47], a[188:191], v[188:191], v[32:47]\n"
"ds_read_b128 v[204:207], v116 offset:19456\n"
"v_max3_u32 v92, v92, v184, v188\n"
"s_waitcnt lgkmcnt(3)\n"
"v_mfma_f32_32x32x16_bf16 v[32:47], a[192:195], v[192:195], v[32:47]\n"
"ds_read_b128 v[208:211], v116 offset:20480\n"
"s_waitcnt lgkmcnt(3)\n"
"v_mfma_f32_32x32x16_bf16 v[32:47], a[196:199], v[196:199], v[32:47]\n"
"ds_read_b128 v[212:215], v116 offset:21504\n"
"v_max3_u32 v92, v92, v192, v196\n"
"s_waitcnt lgkmcnt(3)\n"
"v_mfma_f32_32x32x16_bf16 v[32:47], a[200:203], v[200:203], v[32:47]\n"
"ds_read_b128 v[216:219], v116 offset:22528\n"
"s_waitcnt lgkmcnt(3)\n"
"v_mfma_f32_32x32x16_bf16 v[32:47], a[204:207], v[204:207], v[32:47]\n"
"ds_read_b128 v[220:223], v116 offset:23552\n"
"v_max3_u32 v92, v92, v200, v204\n"
"s_waitcnt lgkmcnt(3)\n"
"v_mfma_f32_32x32x16_bf16 v[32:47], a[208:211], v[208:211], v[32:47]\n"
"ds_read_b128 v[224:227], v116 offset:24576\n"
"s_waitcnt lgkmcnt(3)\n"
"v_mfma_f32_32x32x16_bf16 v[32:47], a[212:215], v[212:215], v[32:47]\n"
"ds_read_b128 v[228:231], v116 offset:25600\n"
"v_max3_u32 v92, v92, v208, v212\n"
"s_waitcnt lgkmcnt(3)\n"
"v_mfma_f32_32x32x16_bf16 v[32:47], a[216:219], v[216:219], v[32:47]\n"
"ds_read_b128 v[232:235], v116 offset:26624\n"
"s_waitcnt lgkmcnt(3)\n"
"v_mfma_f32_32x32x16_bf16 v[32:47], a[220:223], v[220:223], v[32:47]\n"
"ds_read_b128 v[236:239], v116 offset:27648\n"
"v_max3_u32 v92, v92, v216, v220\n"
"s_waitcnt lgkmcnt(3)\n"
"v_mfma_f32_32x32x16_bf16 v[32:47], a[224:227], v[224:227], v[32:47]\n"
"ds_read_b128 v[240:243], v116 offset:28672\n"
"s_waitcnt lgkmcnt(3)\n"
"v_mfma_f32_32x32x16_bf16 v[32:47], a[228:231], v[228:231], v[32:47]\n"
"ds_read_b128 v[244:247], v116 offset:29696\n"
"v_max3_u32 v92, v92, v224, v228\n"
"s_waitcnt lgkmcnt(3)\n"
"v_mfma_f32_32x32x16_bf16 v[32:47], a[232:235], v[232:235], v[32:47]\n"
"ds_read_b128 v[248:251], v116 offset:30720\n"
"s_waitcnt lgkmcnt(3)\n"
"v_mfma_f32_32x32x16_bf16 v[32:47], a[236:239], v[236:239], v[32:47]\n"
"ds_read_b128 v[252:255], v116 offset:31744\n"
"v_max3_u32 v92, v92, v232, v236\n"
"s_waitcnt lgkmcnt(3)\n"
"v_mfma_f32_32x32x16_bf16 v[32:47], a[240:243], v[240:243], v[32:47]\n"
"s_waitcnt lgkmcnt(2)\n"
"v_mfma_f32_32x32x16_bf16 v[32:47], a[244:247], v[244:247], v[32:47]\n"
"v_max3_u32 v92, v92, v240, v244\n"
"s_waitcnt lgkmcnt(1)\n"
"v_mfma_f32_32x32x16_bf16 v[32:47], a[248:251], v[248:251], v[32:47]\n"
"s_waitcnt lgkmcnt(0)\n"
"v_mfma_f32_32x32x16_bf16 v[32:47], a[252:255], v[252:255], v[32:47]\n"
"v_max3_u32 v92, v92, v248, v252\n"
"v_cmp_eq_u32 vcc, -1, v92\n"
"s_cbranch_vccnz L_fail_o1\n"
"s_branch L_ok_o1\n"
"L_fail_o2:\n"
"s_nop 8\n"
"v_lshlrev_b32 v32, 16, v104\n"
"v_and_b32 v33, 0xffff0000, v104\n"
"v_lshlrev_b32 v34, 16, v105\n"
"v_and_b32 v35, 0xffff0000, v105\n"
"v_lshlrev_b32 v36, 16, v106\n"
"v_and_b32 v37, 0xffff0000, v106\n"
"v_lshlrev_b32 v38, 16, v107\n"
"v_and_b32 v39, 0xffff0000, v107\n"
"v_lshlrev_b32 v40, 16, v108\n"
"v_and_b32 v41, 0xffff0000, v108\n"
"v_lshlrev_b32 v42, 16, v109\n"
"v_and_b32 v43, 0xffff0000, v109\n"
"v_lshlrev_b32 v44, 16, v110\n"
"v_and_b32 v45, 0xffff0000, v110\n"
"v_lshlrev_b32 v46, 16, v111\n"
"v_and_b32 v47, 0xffff0000, v111\n"
"s_waitcnt lgkmcnt(0)\n"
"s_barrier\n"
"s_mov_b32 m0, s51\n"
"s_nop 0\n"
"buffer_load_dwordx4 v115, s[8:11], s45 offen offset:0 sc1 lds\n"
"buffer_load_dwordx4 v115, s[8:11], s45 offen offset:1024 sc1 lds\n"
"buffer_load_dwordx4 v115, s[8:11], s45 offen offset:2048 sc1 lds\n"
"buffer_load_dwordx4 v115, s[8:11], s45 offen offset:3072 sc1 lds\n"
"s_add_u32 m0, s51, 16384\n"
"s_add_u32 s35, s45, 16384\n"
"buffer_load_dwordx4 v115, s[8:11], s35 offen offset:0 sc1 lds\n"
"buffer_load_dwordx4 v115, s[8:11], s35 offen offset:1024 sc1 lds\n"
"buffer_load_dwordx4 v115, s[8:11], s35 offen offset:2048 sc1 lds\n"
"buffer_load_dwordx4 v115, s[8:11], s35 offen offset:3072 sc1 lds\n"
"s_waitcnt vmcnt(0)\n"
"s_waitcnt lgkmcnt(0)\n"
"s_barrier\n"
"ds_read_b128 v[128:131], v116 offset:0\n"
"ds_read_b128 v[132:135], v116 offset:1024\n"
"ds_read_b128 v[136:139], v116 offset:2048\n"
"ds_read_b128 v[140:143], v116 offset:3072\n"
"v_mov_b32 v92, 0\n"
"s_waitcnt lgkmcnt(3)\n"
"v_mfma_f32_32x32x16_bf16 v[32:47], a[0:3], v[128:131], v[32:47]\n"
"ds_read_b128 v[144:147], v116 offset:4096\n"
"s_waitcnt lgkmcnt(3)\n"
"v_mfma_f32_32x32x16_bf16 v[32:47], a[4:7], v[132:135], v[32:47]\n"
"ds_read_b128 v[148:151], v116 offset:5120\n"
"v_max3_u32 v92, v92, v128, v132\n"
"s_waitcnt lgkmcnt(3)\n"
"v_mfma_f32_32x32x16_bf16 v[32:47], a[8:11], v[136:139], v[32:47]\n"
"ds_read_b128 v[152:155], v116 offset:6144\n"
"s_waitcnt lgkmcnt(3)\n"
"v_mfma_f32_32x32x16_bf16 v[32:47], a[12:15], v[140:143], v[32:47]\n"
"ds_read_b128 v[156:159], v116 offset:7168\n"
"v_max3_u32 v92, v92, v136, v140\n"
"s_waitcnt lgkmcnt(3)\n"
"v_mfma_f32_32x32x16_bf16 v[32:47], a[16:19], v[144:147], v[32:47]\n"
"ds_read_b128 v[160:163], v116 offset:8192\n"
"s_waitcnt lgkmcnt(3)\n"
"v_mfma_f32_32x32x16_bf16 v[32:47], a[20:23], v[148:151], v[32:47]\n"
"ds_read_b128 v[164:167], v116 offset:9216\n"
"v_max3_u32 v92, v92, v144, v148\n"
"s_waitcnt lgkmcnt(3)\n"
"v_mfma_f32_32x32x16_bf16 v[32:47], a[24:27], v[152:155], v[32:47]\n"
"ds_read_b128 v[168:171], v116 offset:10240\n"
"s_waitcnt lgkmcnt(3)\n"
"v_mfma_f32_32x32x16_bf16 v[32:47], a[28:31], v[156:159], v[32:47]\n"
"ds_read_b128 v[172:175], v116 offset:11264\n"
"v_max3_u32 v92, v92, v152, v156\n"
"s_waitcnt lgkmcnt(3)\n"
"v_mfma_f32_32x32x16_bf16 v[32:47], a[32:35], v[160:163], v[32:47]\n"
"ds_read_b128 v[176:179], v116 offset:12288\n"
"s_waitcnt lgkmcnt(3)\n"
"v_mfma_f32_32x32x16_bf16 v[32:47], a[36:39], v[164:167], v[32:47]\n"
"ds_read_b128 v[180:183], v116 offset:13312\n"
"v_max3_u32 v92, v92, v160, v164\n"
"s_waitcnt lgkmcnt(3)\n"
"v_mfma_f32_32x32x16_bf16 v[32:47], a[40:43], v[168:171], v[32:47]\n"
"ds_read_b128 v[184:187], v116 offset:14336\n"
"s_waitcnt lgkmcnt(3)\n"
"v_mfma_f32_32x32x16_bf16 v[32:47], a[44:47], v[172:175], v[32:47]\n"
"ds_read_b128 v[188:191], v116 offset:15360\n"
"v_max3_u32 v92, v92, v168, v172\n"
"s_waitcnt lgkmcnt(3)\n"
"v_mfma_f32_32x32x16_bf16 v[32:47], a[48:51], v[176:179], v[32:47]\n"
"ds_read_b128 v[192:195], v116 offset:16384\n"
"s_waitcnt lgkmcnt(3)\n"
"v_mfma_f32_32x32x16_bf16 v[32:47], a[52:55], v[180:183], v[32:47]\n"
"ds_read_b128 v[196:199], v116 offset:17408\n"
"v_max3_u32 v92, v92, v176, v180\n"
"s_waitcnt lgkmcnt(3)\n"
"v_mfma_f32_32x32x16_bf16 v[32:47], a[56:59], v[184:187], v[32:47]\n"
"ds_read_b128 v[200:203], v116 offset:18432\n"
"s_waitcnt lgkmcnt(3)\n"
"v_mfma_f32_32x32x16_bf16 v[32:47], a[60:63], v[188:191], v[32:47]\n"
"ds_read_b128 v[204:207], v116 offset:19456\n"
"v_max3_u32 v92, v92, v184, v188\n"
"s_waitcnt lgkmcnt(3)\n"
"v_mfma_f32_32x32x16_bf16 v[32:47], a[64:67], v[192:195], v[32:47]\n"
"ds_read_b128 v[208:211], v116 offset:20480\n"
"s_waitcnt lgkmcnt(3)\n"
"v_mfma_f32_32x32x16_bf16 v[32:47], a[68:71], v[196:199], v[32:47]\n"
"ds_read_b128 v[212:215], v116 offset:21504\n"
"v_max3_u32 v92, v92, v192, v196\n"
"s_waitcnt lgkmcnt(3)\n"
"v_mfma_f32_32x32x16_bf16 v[32:47], a[72:75], v[200:203], v[32:47]\n"
"ds_read_b128 v[216:219], v116 offset:22528\n"
"s_waitcnt lgkmcnt(3)\n"
"v_mfma_f32_32x32x16_bf16 v[32:47], a[76:79], v[204:207], v[32:47]\n"
"ds_read_b128 v[220:223], v116 offset:23552\n"
"v_max3_u32 v92, v92, v200, v204\n"
"s_waitcnt lgkmcnt(3)\n"
"v_mfma_f32_32x32x16_bf16 v[32:47], a[80:83], v[208:211], v[32:47]\n"
"ds_read_b128 v[224:227], v116 offset:24576\n"
"s_waitcnt lgkmcnt(3)\n"
"v_mfma_f32_32x32x16_bf16 v[32:47], a[84:87], v[212:215], v[32:47]\n"
"ds_read_b128 v[228:231], v116 offset:25600\n"
"v_max3_u32 v92, v92, v208, v212\n"
"s_waitcnt lgkmcnt(3)\n"
"v_mfma_f32_32x32x16_bf16 v[32:47], a[88:91], v[216:219], v[32:47]\n"
"ds_read_b128 v[232:235], v116 offset:26624\n"
"s_waitcnt lgkmcnt(3)\n"
"v_mfma_f32_32x32x16_bf16 v[32:47], a[92:95], v[220:223], v[32:47]\n"
"ds_read_b128 v[236:239], v116 offset:27648\n"
"v_max3_u32 v92, v92, v216, v220\n"
"s_waitcnt lgkmcnt(3)\n"
"v_mfma_f32_32x32x16_bf16 v[32:47], a[96:99], v[224:227], v[32:47]\n"
"ds_read_b128 v[240:243], v116 offset:28672\n"
"s_waitcnt lgkmcnt(3)\n"
"v_mfma_f32_32x32x16_bf16 v[32:47], a[100:103], v[228:231], v[32:47]\n"
"ds_read_b128 v[244:247], v116 offset:29696\n"
"v_max3_u32 v92, v92, v224, v228\n"
"s_waitcnt lgkmcnt(3)\n"
"v_mfma_f32_32x32x16_bf16 v[32:47], a[104:107], v[232:235], v[32:47]\n"
"ds_read_b128 v[248:251], v116 offset:30720\n"
"s_waitcnt lgkmcnt(3)\n"
"v_mfma_f32_32x32x16_bf16 v[32:47], a[108:111], v[236:239], v[32:47]\n"
"ds_read_b128 v[252:255], v116 offset:31744\n"
"v_max3_u32 v92, v92, v232, v236\n"
"s_waitcnt lgkmcnt(3)\n"
"v_mfma_f32_32x32x16_bf16 v[32:47], a[112:115], v[240:243], v[32:47]\n"
"s_waitcnt lgkmcnt(2)\n"
"v_mfma_f32_32x32x16_bf16 v[32:47], a[116:119], v[244:247], v[32:47]\n"
"v_max3_u32 v92, v92, v240, v244\n"
"s_waitcnt lgkmcnt(1)\n"
"v_mfma_f32_32x32x16_bf16 v[32:47], a[120:123], v[248:251], v[32:47]\n"
"s_waitcnt lgkmcnt(0)\n"
"v_mfma_f32_32x32x16_bf16 v[32:47], a[124:127], v[252:255], v[32:47]\n"
"v_max3_u32 v92, v92, v248, v252\n"
"v_cmp_eq_u32 vcc, -1, v92\n"
"s_cbranch_vccnz L_fail_o2\n"
"s_branch L_ok_o2\n"
"L_fail_o3:\n"
"s_nop 8\n"
"v_lshlrev_b32 v32, 16, v104\n"
"v_and_b32 v33, 0xffff0000, v104\n"
"v_lshlrev_b32 v34, 16, v105\n"
"v_and_b32 v35, 0xffff0000, v105\n"
"v_lshlrev_b32 v36, 16, v106\n"
"v_and_b32 v37, 0xffff0000, v106\n"
"v_lshlrev_b32 v38, 16, v107\n"
"v_and_b32 v39, 0xffff0000, v107\n"
"v_lshlrev_b32 v40, 16, v108\n"
"v_and_b32 v41, 0xffff0000, v108\n"
"v_lshlrev_b32 v42, 16, v109\n"
"v_and_b32 v43, 0xffff0000, v109\n"
"v_lshlrev_b32 v44, 16, v110\n"
"v_and_b32 v45, 0xffff0000, v110\n"
"v_lshlrev_b32 v46, 16, v111\n"
"v_and_b32 v47, 0xffff0000, v111\n"
"s_waitcnt lgkmcnt(0)\n"
"s_barrier\n"
"s_mov_b32 m0, s51\n"
"s_nop 0\n"
"buffer_load_dwordx4 v115, s[8:11], s45 offen offset:0 sc1 lds\n"
"buffer_load_dwordx4 v115, s[8:11], s45 offen offset:1024 sc1 lds\n"
"buffer_load_dwordx4 v115, s[8:11], s45 offen offset:2048 sc1 lds\n"
"buffer_load_dwordx4 v115, s[8:11], s45 offen offset:3072 sc1 lds\n"
"s_add_u32 m0, s51, 16384\n"
"s_add_u32 s35, s45, 16384\n"
"buffer_load_dwordx4 v115, s[8:11], s35 offen offset:0 sc1 lds\n"
"buffer_load_dwordx4 v115, s[8:11], s35 offen offset:1024 sc1 lds\n"
"buffer_load_dwordx4 v115, s[8:11], s35 offen offset:2048 sc1 lds\n"
"buffer_load_dwordx4 v115, s[8:11], s35 offen offset:3072 sc1 lds\n"
"s_waitcnt vmcnt(0)\n"
"s_waitcnt lgkmcnt(0)\n"
"s_barrier\n"
"ds_read_b128 v[128:131], v116 offset:0\n"
"ds_read_b128 v[132:135], v116 offset:1024\n"
"ds_read_b128 v[136:139], v116 offset:2048\n"
"ds_read_b128 v[140:143], v116 offset:3072\n"
"v_mov_b32 v92, 0\n"
"s_waitcnt lgkmcnt(3)\n"
"v_mfma_f32_32x32x16_bf16 v[32:47], a[128:131], v[128:131], v[32:47]\n"
"ds_read_b128 v[144:147], v116 offset:4096\n"
"s_waitcnt lgkmcnt(3)\n"
"v_mfma_f32_32x32x16_bf16 v[32:47], a[132:135], v[132:135], v[32:47]\n"
"ds_read_b128 v[148:151], v116 offset:5120\n"
"v_max3_u32 v92, v92, v128, v132\n"
"s_waitcnt lgkmcnt(3)\n"
"v_mfma_f32_32x32x16_bf16 v[32:47], a[136:139], v[136:139], v[32:47]\n"
"ds_read_b128 v[152:155], v116 offset:6144\n"
"s_waitcnt lgkmcnt(3)\n"
"v_mfma_f32_32x32x16_bf16 v[32:47], a[140:143], v[140:143], v[32:47]\n"
"ds_read_b128 v[156:159], v116 offset:7168\n"
"v_max3_u32 v92, v92, v136, v140\n"
"s_waitcnt lgkmcnt(3)\n"
"v_mfma_f32_32x32x16_bf16 v[32:47], a[144:147], v[144:147], v[32:47]\n"
"ds_read_b128 v[160:163], v116 offset:8192\n"
"s_waitcnt lgkmcnt(3)\n"
"v_mfma_f32_32x32x16_bf16 v[32:47], a[148:151], v[148:151], v[32:47]\n"
"ds_read_b128 v[164:167], v116 offset:9216\n"
"v_max3_u32 v92, v92, v144, v148\n"
"s_waitcnt lgkmcnt(3)\n"
"v_mfma_f32_32x32x16_bf16 v[32:47], a[152:155], v[152:155], v[32:47]\n"
"ds_read_b128 v[168:171], v116 offset:10240\n"
"s_waitcnt lgkmcnt(3)\n"
"v_mfma_f32_32x32x16_bf16 v[32:47], a[156:159], v[156:159], v[32:47]\n"
"ds_read_b128 v[172:175], v116 offset:11264\n"
"v_max3_u32 v92, v92, v152, v156\n"
"s_waitcnt lgkmcnt(3)\n"
"v_mfma_f32_32x32x16_bf16 v[32:47], a[160:163], v[160:163], v[32:47]\n"
"ds_read_b128 v[176:179], v116 offset:12288\n"
"s_waitcnt lgkmcnt(3)\n"
"v_mfma_f32_32x32x16_bf16 v[32:47], a[164:167], v[164:167], v[32:47]\n"
"ds_read_b128 v[180:183], v116 offset:13312\n"
"v_max3_u32 v92, v92, v160, v164\n"
"s_waitcnt lgkmcnt(3)\n"
"v_mfma_f32_32x32x16_bf16 v[32:47], a[168:171], v[168:171], v[32:47]\n"
"ds_read_b128 v[184:187], v116 offset:14336\n"
"s_waitcnt lgkmcnt(3)\n"
"v_mfma_f32_32x32x16_bf16 v[32:47], a[172:175], v[172:175], v[32:47]\n"
"ds_read_b128 v[188:191], v116 offset:15360\n"
"v_max3_u32 v92, v92, v168, v172\n"
"s_waitcnt lgkmcnt(3)\n"
"v_mfma_f32_32x32x16_bf16 v[32:47], a[176:179], v[176:179], v[32:47]\n"
"ds_read_b128 v[192:195], v116 offset:16384\n"
"s_waitcnt lgkmcnt(3)\n"
"v_mfma_f32_32x32x16_bf16 v[32:47], a[180:183], v[180:183], v[32:47]\n"
"ds_read_b128 v[196:199], v116 offset:17408\n"
"v_max3_u32 v92, v92, v176, v180\n"
"s_waitcnt lgkmcnt(3)\n"
"v_mfma_f32_32x32x16_bf16 v[32:47], a[184:187], v[184:187], v[32:47]\n"
"ds_read_b128 v[200:203], v116 offset:18432\n"
"s_waitcnt lgkmcnt(3)\n"
"v_mfma_f32_32x32x16_bf16 v[32:47], a[188:191], v[188:191], v[32:47]\n"
"ds_read_b128 v[204:207], v116 offset:19456\n"
"v_max3_u32 v92, v92, v184, v188\n"
"s_waitcnt lgkmcnt(3)\n"
"v_mfma_f32_32x32x16_bf16 v[32:47], a[192:195], v[192:195], v[32:47]\n"
"ds_read_b128 v[208:211], v116 offset:20480\n"
"s_waitcnt lgkmcnt(3)\n"
"v_mfma_f32_32x32x16_bf16 v[32:47], a[196:199], v[196:199], v[32:47]\n"
"ds_read_b128 v[212:215], v116 offset:21504\n"
"v_max3_u32 v92, v92, v192, v196\n"
"s_waitcnt lgkmcnt(3)\n"
"v_mfma_f32_32x32x16_bf16 v[32:47], a[200:203], v[200:203], v[32:47]\n"
"ds_read_b128 v[216:219], v116 offset:22528\n"
"s_waitcnt lgkmcnt(3)\n"
"v_mfma_f32_32x32x16_bf16 v[32:47], a[204:207], v[204:207], v[32:47]\n"
"ds_read_b128 v[220:223], v116 offset:23552\n"
"v_max3_u32 v92, v92, v200, v204\n"
"s_waitcnt lgkmcnt(3)\n"
"v_mfma_f32_32x32x16_bf16 v[32:47], a[208:211], v[208:211], v[32:47]\n"
"ds_read_b128 v[224:227], v116 offset:24576\n"
"s_waitcnt lgkmcnt(3)\n"
"v_mfma_f32_32x32x16_bf16 v[32:47], a[212:215], v[212:215], v[32:47]\n"
"ds_read_b128 v[228:231], v116 offset:25600\n"
"v_max3_u32 v92, v92, v208, v212\n"
"s_waitcnt lgkmcnt(3)\n"
"v_mfma_f32_32x32x16_bf16 v[32:47], a[216:219], v[216:219], v[32:47]\n"
"ds_read_b128 v[232:235], v116 offset:26624\n"
"s_waitcnt lgkmcnt(3)\n"
"v_mfma_f32_32x32x16_bf16 v[32:47], a[220:223], v[220:223], v[32:47]\n"
"ds_read_b128 v[236:239], v116 offset:27648\n"
"v_max3_u32 v92, v92, v216, v220\n"
"s_waitcnt lgkmcnt(3)\n"
"v_mfma_f32_32x32x16_bf16 v[32:47], a[224:227], v[224:227], v[32:47]\n"
"ds_read_b128 v[240:243], v116 offset:28672\n"
"s_waitcnt lgkmcnt(3)\n"
"v_mfma_f32_32x32x16_bf16 v[32:47], a[228:231], v[228:231], v[32:47]\n"
"ds_read_b128 v[244:247], v116 offset:29696\n"
"v_max3_u32 v92, v92, v224, v228\n"
"s_waitcnt lgkmcnt(3)\n"
"v_mfma_f32_32x32x16_bf16 v[32:47], a[232:235], v[232:235], v[32:47]\n"
"ds_read_b128 v[248:251], v116 offset:30720\n"
"s_waitcnt lgkmcnt(3)\n"
"v_mfma_f32_32x32x16_bf16 v[32:47], a[236:239], v[236:239], v[32:47]\n"
"ds_read_b128 v[252:255], v116 offset:31744\n"
"v_max3_u32 v92, v92, v232, v236\n"
"s_waitcnt lgkmcnt(3)\n"
"v_mfma_f32_32x32x16_bf16 v[32:47], a[240:243], v[240:243], v[32:47]\n"
"s_waitcnt lgkmcnt(2)\n"
"v_mfma_f32_32x32x16_bf16 v[32:47], a[244:247], v[244:247], v[32:47]\n"
"v_max3_u32 v92, v92, v240, v244\n"
"s_waitcnt lgkmcnt(1)\n"
"v_mfma_f32_32x32x16_bf16 v[32:47], a[248:251], v[248:251], v[32:47]\n"
"s_waitcnt lgkmcnt(0)\n"
"v_mfma_f32_32x32x16_bf16 v[32:47], a[252:255], v[252:255], v[32:47]\n"
"v_max3_u32 v92, v92, v248, v252\n"
"v_cmp_eq_u32 vcc, -1, v92\n"
"s_cbranch_vccnz L_fail_o3\n"
"s_branch L_ok_o3\n"
"L_end:\n"
"s_nop 0\n"
  :: "s"(__builtin_amdgcn_kernarg_segment_ptr()), "s"(blockIdx.x), "v"(threadIdx.x), "s"((unsigned)(size_t)(__attribute__((address_space(3))) char*)smem)
  : "v1", "v2", "v3", "v4", "v5", "v6", "v7", "v8", "v9", "v10", "v11", "v12", "v13", "v14", "v15", "v16", "v17", "v18", "v19", "v20", "v21", "v22", "v23", "v24", "v25", "v26", "v27", "v28", "v29", "v30", "v31", "v32", "v33", "v34", "v35", "v36", "v37", "v38", "v39", "v40", "v41", "v42", "v43", "v44", "v45", "v46", "v47", "v48", "v49", "v50", "v51", "v52", "v53", "v54", "v55", "v56", "v57", "v58", "v59", "v60", "v61", "v62", "v63", "v64", "v65", "v66", "v67", "v68", "v69", "v70", "v71", "v72", "v73", "v74", "v75", "v76", "v77", "v78", "v79", "v80", "v81", "v82", "v83", "v84", "v85", "v86", "v87", "v88", "v89", "v90", "v91", "v92", "v93", "v94", "v95", "v96", "v97", "v98", "v99", "v100", "v101", "v102", "v103", "v104", "v105", "v106", "v107", "v108", "v109", "v110", "v111", "v112", "v113", "v114", "v115", "v116", "v117", "v118", "v119", "v120", "v121", "v122", "v123", "v124", "v125", "v126", "v127", "v128", "v129", "v130", "v131", "v132", "v133", "v134", "v135", "v136", "v137", "v138", "v139", "v140", "v141", "v142", "v143", "v144", "v145", "v146", "v147", "v148", "v149", "v150", "v151", "v152", "v153", "v154", "v155", "v156", "v157", "v158", "v159", "v160", "v161", "v162", "v163", "v164", "v165", "v166", "v167", "v168", "v169", "v170", "v171", "v172", "v173", "v174", "v175", "v176", "v177", "v178", "v179", "v180", "v181", "v182", "v183", "v184", "v185", "v186", "v187", "v188", "v189", "v190", "v191", "v192", "v193", "v194", "v195", "v196", "v197", "v198", "v199", "v200", "v201", "v202", "v203", "v204", "v205", "v206", "v207", "v208", "v209", "v210", "v211", "v212", "v213", "v214", "v215", "v216", "v217", "v218", "v219", "v220", "v221", "v222", "v223", "v224", "v225", "v226", "v227", "v228", "v229", "v230", "v231", "v232", "v233", "v234", "v235", "v236", "v237", "v238", "v239", "v240", "v241", "v242", "v243", "v244", "v245", "v246", "v247", "v248", "v249", "v250", "v251", "v252", "v253", "v254", "v255", "a0", "a1", "a2", "a3", "a4", "a5", "a6", "a7", "a8", "a9", "a10", "a11", "a12", "a13", "a14", "a15", "a16", "a17", "a18", "a19", "a20", "a21", "a22", "a23", "a24", "a25", "a26", "a27", "a28", "a29", "a30", "a31", "a32", "a33", "a34", "a35", "a36", "a37", "a38", "a39", "a40", "a41", "a42", "a43", "a44", "a45", "a46", "a47", "a48", "a49", "a50", "a51", "a52", "a53", "a54", "a55", "a56", "a57", "a58", "a59", "a60", "a61", "a62", "a63", "a64", "a65", "a66", "a67", "a68", "a69", "a70", "a71", "a72", "a73", "a74", "a75", "a76", "a77", "a78", "a79", "a80", "a81", "a82", "a83", "a84", "a85", "a86", "a87", "a88", "a89", "a90", "a91", "a92", "a93", "a94", "a95", "a96", "a97", "a98", "a99", "a100", "a101", "a102", "a103", "a104", "a105", "a106", "a107", "a108", "a109", "a110", "a111", "a112", "a113", "a114", "a115", "a116", "a117", "a118", "a119", "a120", "a121", "a122", "a123", "a124", "a125", "a126", "a127", "a128", "a129", "a130", "a131", "a132", "a133", "a134", "a135", "a136", "a137", "a138", "a139", "a140", "a141", "a142", "a143", "a144", "a145", "a146", "a147", "a148", "a149", "a150", "a151", "a152", "a153", "a154", "a155", "a156", "a157", "a158", "a159", "a160", "a161", "a162", "a163", "a164", "a165", "a166", "a167", "a168", "a169", "a170", "a171", "a172", "a173", "a174", "a175", "a176", "a177", "a178", "a179", "a180", "a181", "a182", "a183", "a184", "a185", "a186", "a187", "a188", "a189", "a190", "a191", "a192", "a193", "a194", "a195", "a196", "a197", "a198", "a199", "a200", "a201", "a202", "a203", "a204", "a205", "a206", "a207", "a208", "a209", "a210", "a211", "a212", "a213", "a214", "a215", "a216", "a217", "a218", "a219", "a220", "a221", "a222", "a223", "a224", "a225", "a226", "a227", "a228", "a229", "a230", "a231", "a232", "a233", "a234", "a235", "a236", "a237", "a238", "a239", "a240", "a241", "a242", "a243", "a244", "a245", "a246", "a247", "a248", "a249", "a250", "a251", "a252", "a253", "a254", "a255", "s4", "s5", "s6", "s7", "s8", "s9", "s10", "s11", "s12", "s13", "s14", "s15", "s16", "s17", "s18", "s19", "s20", "s21", "s22", "s23", "s24", "s25", "s26", "s27", "s28", "s29", "s30", "s31", "s33", "s34", "s35", "s36", "s37", "s38", "s39", "s40", "s41", "s42", "s43", "s44", "s45", "s46", "s47", "s48", "s49", "s50", "s51", "s52", "s53", "s54", "s55", "s56", "s57", "s58", "s59", "s60", "s61", "s62", "s63", "s64", "s65", "s66", "s67", "s68", "s69", "s70", "s71", "s72", "s73", "s74", "s75", "s76", "s77", "s78", "s79", "s80", "s81", "s82", "s83", "s84", "s85", "s86", "s87", "s88", "s89", "s90", "s91", "s92", "s93", "s94", "s95", "s96", "s97", "s98", "s99", "s100", "s101", "vcc", "scc", "memory");
}

extern "C" void kernel_launch(void* const* d_in, const int* in_sizes, int n_in, void* d_out, int out_size, void* d_ws, size_t ws_size,
                              hipStream_t stream) {
  Params p;
  p.x0 = d_in[0]; p.wq0 = d_in[1]; p.wo0 = d_in[2]; p.wq1 = d_in[4]; p.wo1 = d_in[5];
  p.out = d_out; p.ws = d_ws;
  hipLaunchKernelGGL(fused_kernel, dim3(256), dim3(256), 0, stream, p);
}
```
